```python
import torch

def _softmax(x, axis=-1):
    return torch.softmax(x, dim=axis)

def _gelu(x):
    return torch.nn.functional.gelu(x, approximate='tanh')

def _silu(x):
    return torch.nn.functional.silu(x)

def _sigmoid(x):
    return torch.sigmoid(x)

def _layernorm(x, axis=-1, eps=1e-05):
    m = x.mean(dim=axis, keepdim=True)
    v = x.var(dim=axis, keepdim=True, unbiased=False)
    return (x - m) / torch.sqrt(v + eps)

def _scatter_add(tgt, idx, src, axis=0):
    return tgt.clone().index_add_(axis, idx, src)

def _segment_sum(src, seg, n):
    out = torch.zeros((n,) + src.shape[1:], dtype=src.dtype)
    return out.index_add_(0, seg, src)

def _pad(x, pw):
    flat = tuple((v for pair in reversed(pw) for v in pair))
    return torch.nn.functional.pad(x, flat)
kernel_name = 'adaLN_cond__compressed_kv_cla__glu_gated_reglu__prompt'

def setup_inputs():

    _w = lambda a: (lambda t: t.to(torch.bfloat16) if t.is_floating_point() else t)(torch.as_tensor(a))

    def _A_setup():
        B, H, Sq, Sk, D, M = (1, 32, 2048, 2048, 128, 512)
        Dc = 4 * D
        return {'x': _w((lambda *s: torch.randn(*s, dtype=torch.bfloat16) * 0.05)(B, Sq, M)), 'c_kv': _w((lambda *s: torch.randn(*s, dtype=torch.bfloat16) * 0.8)(B, Sk, Dc)), 'w_q': _w((lambda *s: torch.randn(*s, dtype=torch.bfloat16) * 1.6)(M, H, D) * M ** (-0.5)), 'w_uk': _w((lambda *s: torch.randn(*s, dtype=torch.bfloat16) * 1.6)(Dc, H, D) * Dc ** (-0.5)), 'w_uv': _w((lambda *s: torch.randn(*s, dtype=torch.bfloat16) * 0.8)(Dc, H, D) * Dc ** (-0.5)), 'w_o': _w((lambda *s: torch.randn(*s, dtype=torch.bfloat16) * 0.8)(H, D, M) * (H * D) ** (-0.5))}
    _A_in = _A_setup()

    def _B_setup():
        B, S, D, F = (1, 2048, 512, 1792)
        return {'x': _w((lambda *s: torch.randn(*s, dtype=torch.bfloat16) * 0.05)(B, S, D)), 'W_gate': _w((lambda *s: torch.randn(*s, dtype=torch.bfloat16) * 0.4)(D, F) * (2.0 / D) ** 0.5), 'W_up': _w((lambda *s: torch.randn(*s, dtype=torch.bfloat16) * 0.4)(D, F) * (2.0 / D) ** 0.5), 'W_down': _w((lambda *s: torch.randn(*s, dtype=torch.bfloat16) * 0.4)(F, D) * (1.0 / F) ** 0.5)}
    _B_in = _B_setup()
    return {'A_x': _A_in['x'], 'A_c_kv': _A_in['c_kv'], 'A_w_q': _A_in['w_q'], 'A_w_uk': _A_in['w_uk'], 'A_w_uv': _A_in['w_uv'], 'A_w_o': _A_in['w_o'], 'B_x': _B_in['x'], 'B_W_gate': _B_in['W_gate'], 'B_W_up': _B_in['W_up'], 'B_W_down': _B_in['W_down'], 'c': _w((lambda *s: torch.randn(*s, dtype=torch.bfloat16) * 0.1)(1, 512) * 8.0), 'w_mod': _w((lambda *s: torch.randn(*s, dtype=torch.bfloat16) * 0.1)(512, 6 * 512) * 512 ** (-0.5) * 8.0)}

def reference(A_x, A_c_kv, A_w_q, A_w_uk, A_w_uv, A_w_o, B_x, B_W_gate, B_W_up, B_W_down, c, w_mod):

    def _A_ref(x, c_kv, w_q, w_uk, w_uv, w_o):
        B, Sq, M = (x.shape[0], x.shape[1], x.shape[2])
        Sk = c_kv.shape[1]
        H, D = (w_q.shape[1], w_q.shape[2])
        q = torch.einsum('bqm,mhd->bhqd', x, w_q)
        k = torch.einsum('bkc,chd->bhkd', c_kv, w_uk)
        v = torch.einsum('bkc,chd->bhkd', c_kv, w_uv)
        scores = torch.einsum('bhqd,bhkd->bhqk', q, k) * D ** (-0.5)
        off = Sk - Sq
        qi = torch.arange(Sq)[:, None]
        kj = torch.arange(Sk)[None, :]
        cmask = kj <= qi + off
        scores = torch.where(cmask[None, None], scores, -1000000000.0)
        p = _softmax(scores, axis=-1)
        ctx = torch.einsum('bhqk,bhkd->bhqd', p, v)
        return torch.einsum('bhqd,hdm->bqm', ctx, w_o)

    def _B_ref(x, W_gate, W_up, W_down):
        gate = torch.einsum('bsd,df->bsf', x, W_gate)
        up = torch.einsum('bsd,df->bsf', x, W_up)
        gate = gate * (lambda x, dt: x.to(dt))(gate > 0, gate.dtype)
        h = gate * up
        return torch.einsum('bsf,fd->bsd', h, W_down)
    x = A_x
    mod = (_silu(c) @ w_mod).reshape(c.shape[0], 6, 1, -1)
    sa, ca, ga, sb, cb, gb = (mod[:, 0].clone(), mod[:, 1].clone(), mod[:, 2].clone(), mod[:, 3].clone(), mod[:, 4].clone(), mod[:, 5].clone())
    h = _layernorm(x, axis=-1) * (1 + ca) + sa
    x = x + ga * _A_ref(h, A_c_kv, A_w_q, A_w_uk, A_w_uv, A_w_o)
    h = _layernorm(x, axis=-1) * (1 + cb) + sb
    x = x + gb * _B_ref(h, B_W_gate, B_W_up, B_W_down)
    return x
```

```cpp
#include <hip/hip_runtime.h>
#include <hip/hip_bf16.h>
typedef unsigned short u16;
__device__ __forceinline__ float bf2f(u16 b){ return __uint_as_float(((unsigned)b)<<16); }
__device__ __forceinline__ u16 f2bf(float f){ unsigned u=__float_as_uint(f); u += 0x7FFFu + ((u>>16)&1u); return (u16)(u>>16); }

__global__ void k_mod(const u16* c, const u16* wmod, float* mod){
  int n = blockIdx.x*blockDim.x+threadIdx.x; if(n>=3072) return;
  float acc=0; for(int k=0;k<512;++k){ float cv=bf2f(c[k]); float s=cv/(1.f+__expf(-cv)); s=bf2f(f2bf(s)); acc+=s*bf2f(wmod[k*3072+n]); }
  mod[n]=bf2f(f2bf(acc));
}
__global__ void k_ln(const u16* x, const float* shift, const float* scale, u16* h){
  int row=blockIdx.x, l=threadIdx.x; float v[8]; float s=0;
  for(int j=0;j<8;++j){ v[j]=bf2f(x[row*512+l*8+j]); s+=v[j]; }
  for(int o=32;o;o>>=1) s+=__shfl_xor(s,o);
  float m=s/512.f; float q=0; for(int j=0;j<8;++j){ float d=v[j]-m; q+=d*d; }
  for(int o=32;o;o>>=1) q+=__shfl_xor(q,o);
  float r=rsqrtf(q/512.f+1e-5f);
  for(int j=0;j<8;++j){ int k=l*8+j; h[row*512+k]=f2bf((v[j]-m)*r*(1.f+scale[k])+shift[k]); }
}
__global__ void k_gemm(const u16* A, const u16* B, u16* C, int M,int N,int K, int mode, const u16* res, const float* g){
  __shared__ float sa[16][17], sb[16][17];
  int tx=threadIdx.x, ty=threadIdx.y; int row=blockIdx.y*16+ty, col=blockIdx.x*16+tx; float acc=0;
  for(int k0=0;k0<K;k0+=16){ sa[ty][tx]=bf2f(A[(long)row*K+k0+tx]); sb[ty][tx]=bf2f(B[(long)(k0+ty)*N+col]); __syncthreads();
    for(int k=0;k<16;++k) acc+=sa[ty][k]*sb[k][tx]; __syncthreads(); }
  if(mode==0) C[(long)row*N+col]=f2bf(acc); else C[(long)row*N+col]=f2bf(bf2f(res[(long)row*N+col])+g[col]*acc);
}
__global__ void k_glu(const u16* g, const u16* u, u16* h, int n){ int i=blockIdx.x*blockDim.x+threadIdx.x; if(i<n){ float a=bf2f(g[i]); a=a>0?a:0; h[i]=f2bf(a*bf2f(u[i])); } }
__global__ void k_attn(const u16* q, const u16* k, const u16* v, u16* ctx){
  __shared__ float sq[128]; __shared__ float p[2048]; __shared__ float red[128];
  int row=blockIdx.x, h=blockIdx.y, t=threadIdx.x;
  sq[t]=bf2f(q[(long)row*4096+h*128+t]); __syncthreads();
  float mx=-1e30f;
  for(int j=t;j<=row;j+=128){ float s=0; const u16* kr=k+(long)j*4096+h*128; for(int d=0;d<128;++d) s+=sq[d]*bf2f(kr[d]); s*=0.08838834764831845f; p[j]=s; mx=fmaxf(mx,s);}
  red[t]=mx; __syncthreads(); for(int o=64;o;o>>=1){ if(t<o) red[t]=fmaxf(red[t],red[t+o]); __syncthreads(); } mx=red[0]; __syncthreads();
  float sum=0; for(int j=t;j<=row;j+=128){ float e=__expf(p[j]-mx); p[j]=e; sum+=e; }
  red[t]=sum; __syncthreads(); for(int o=64;o;o>>=1){ if(t<o) red[t]+=red[t+o]; __syncthreads(); } sum=red[0];
  float acc=0; for(int j=0;j<=row;++j) acc+=p[j]*bf2f(v[(long)j*4096+h*128+t]);
  ctx[(long)row*4096+h*128+t]=f2bf(acc/sum);
}
extern "C" void kernel_launch(void* const* d_in, const int* in_sizes, int n_in, void* d_out, int out_size, void* d_ws, size_t ws_size, hipStream_t stream){
  const u16* x=(const u16*)d_in[0]; const u16* ckv=(const u16*)d_in[1]; const u16* wq=(const u16*)d_in[2]; const u16* wuk=(const u16*)d_in[3]; const u16* wuv=(const u16*)d_in[4]; const u16* wo=(const u16*)d_in[5];
  const u16* wg=(const u16*)d_in[7]; const u16* wu=(const u16*)d_in[8]; const u16* wd=(const u16*)d_in[9]; const u16* c=(const u16*)d_in[10]; const u16* wmod=(const u16*)d_in[11];
  char* ws=(char*)d_ws; float* mod=(float*)ws; u16* h=(u16*)(ws+(1<<20)); u16* q=(u16*)(ws+(4<<20)); u16* k=(u16*)(ws+(24<<20)); u16* v=(u16*)(ws+(44<<20)); u16* ctx=(u16*)(ws+(64<<20));
  u16* x1=(u16*)(ws+(84<<20)); u16* h2=(u16*)(ws+(88<<20)); u16* g=(u16*)(ws+(92<<20)); u16* u=(u16*)(ws+(100<<20)); u16* hm=(u16*)(ws+(108<<20));
  k_mod<<<12,256,0,stream>>>(c,wmod,mod);
  k_ln<<<2048,64,0,stream>>>(x,mod,mod+512,h);
  dim3 b(16,16);
  k_gemm<<<dim3(256,128),b,0,stream>>>(h,wq,q,2048,4096,512,0,nullptr,nullptr);
  k_gemm<<<dim3(256,128),b,0,stream>>>(ckv,wuk,k,2048,4096,512,0,nullptr,nullptr);
  k_gemm<<<dim3(256,128),b,0,stream>>>(ckv,wuv,v,2048,4096,512,0,nullptr,nullptr);
  k_attn<<<dim3(2048,32),128,0,stream>>>(q,k,v,ctx);
  k_gemm<<<dim3(32,128),b,0,stream>>>(ctx,wo,x1,2048,512,4096,1,x,mod+1024);
  k_ln<<<2048,64,0,stream>>>(x1,mod+1536,mod+2048,h2);
  k_gemm<<<dim3(112,128),b,0,stream>>>(h2,wg,g,2048,1792,512,0,nullptr,nullptr);
  k_gemm<<<dim3(112,128),b,0,stream>>>(h2,wu,u,2048,1792,512,0,nullptr,nullptr);
  k_glu<<<2048*1792/256,256,0,stream>>>(g,u,hm,2048*1792);
  k_gemm<<<dim3(32,128),b,0,stream>>>(hm,wd,(u16*)d_out,2048,512,1792,1,x1,mod+2560);
}
```

```cpp
constexpr int F1_STAGE = 22 * 1024, F1_NST = 6, F1_LDS = F1_STAGE * F1_NST;
template <int WC> DI void ffn1_main(unsigned ldsb, int lane, int wid, __amdgpu_buffer_rsrc_t srdA, __amdgpu_buffer_rsrc_t srdW, unsigned soffA, unsigned soffW,
                                    f32x4 (&ag)[4][2], f32x4 (&au)[4][2], Stamps& st_) {
  constexpr int NF = WC ? 3 : 4, F0 = WC ? 4 : 0;
  const unsigned voffL = lane * 16;
  const int wr = wid & 3;
#define F1_LOAD(u) do { const unsigned dst_ = ldsb + ((u) % F1_NST) * F1_STAGE; \
    dma16(srdA, voffL, soffA + (u) * 1024, dst_ + wid * 1024); dma16(srdW, voffL, soffW + (u) * 14336 + wid * 1024, dst_ + 8192 + wid * 1024); \
    dma16(srdW, voffL, wid < 6 ? soffW + (u) * 14336 + 8192 + wid * 1024 : 0x7fffff00u, wid < 6 ? dst_ + 16384 + wid * 1024 : dst_ + 8192 + wid * 1024 + 0u * 0); } while (0)
#undef F1_LOAD
#define F1_LOAD(u) do { const unsigned dst_ = ldsb + ((u) % F1_NST) * F1_STAGE; \
    dma16(srdA, voffL, soffA + (u) * 1024, dst_ + wid * 1024); dma16(srdW, voffL, soffW + (u) * 14336 + wid * 1024, dst_ + 8192 + wid * 1024); \
    dma16(srdW, voffL, wid < 6 ? soffW + (u) * 14336 + 8192 + wid * 1024 : 0x7fffff00u, wid < 6 ? dst_ + 16384 + wid * 1024 : ldsb + F1_LDS + (wid - 6) * 1024); } while (0)
  F1_LOAD(0); F1_LOAD(1); F1_LOAD(2); F1_LOAD(3);
  const unsigned rdA = ldsb + wr * 2048 + lane * 16, rdW = ldsb + 8192 + F0 * 1024 + lane * 16;
#pragma unroll
  for (int u = 0; u < 16; ++u) {
    if (u + 3 < 16) WAITV_BAR(9); else if (u + 2 < 16) WAITV_BAR(6); else if (u + 1 < 16) WAITV_BAR(3); else WAITV_BAR(0);
    if (u == 0) STAMP(1);
    const unsigned so = (u % F1_NST) * F1_STAGE;
    bf16x8 xa[2], wg[NF], wu[NF];
    xa[0] = lds_rd128(rdA + so); xa[1] = lds_rd128(rdA + so + 1024);
#pragma unroll
    for (int f = 0; f < NF; ++f) { wg[f] = lds_rd128(rdW + so + f * 1024); wu[f] = lds_rd128(rdW + so + 7168 + f * 1024); }
    SBAR();
    if (u + 4 < 16) F1_LOAD(u + 4);
    SBAR();
#pragma unroll
    for (int f = 0; f < NF; ++f)
#pragma unroll
      for (int r = 0; r < 2; ++r) {
        if (u == 0) { mfma_zero(ag[f][r], wg[f], xa[r]); mfma_zero(au[f][r], wu[f], xa[r]); }
        else { mfma_acc(ag[f][r], wg[f], xa[r]); mfma_acc(au[f][r], wu[f], xa[r]); } }
    SBAR();
  }
#undef F1_LOAD
  MFMA_DRAIN();
}
template <int WC> DI void ffn1_epi(const f32x4 (&ag)[4][2], const f32x4 (&au)[4][2], const float* __restrict__ csb, const float (&mu)[2], const float (&rs)[2], char* __restrict__ hm, int nt, int rb0, int lane) {
  constexpr int NF = WC ? 3 : 4, F0 = WC ? 4 : 0;
  const int g = lane >> 4;
#pragma unroll
  for (int f = 0; f < NF; ++f) {
    const int F = nt * 7 + F0 + f, col = F * 16 + 4 * g;
    const f32x4 csg = *(const f32x4*)(csb + col), bg = *(const f32x4*)(csb + 1792 + col), csu = *(const f32x4*)(csb + 3584 + col), bu = *(const f32x4*)(csb + 5376 + col);
#pragma unroll
    for (int r = 0; r < 2; ++r) {
      float h[4];
#pragma unroll
      for (int j = 0; j < 4; ++j) { const float gv = rs[r] * (ag[f][r][j] - mu[r] * csg[j]) + bg[j], uv = rs[r] * (au[f][r][j] - mu[r] * csu[j]) + bu[j]; h[j] = gv > 0.f ? gv * uv : 0.f; }
      *(u32x2*)(hm + ((size_t)((rb0 + r) * 56 + (F >> 1)) * 64 + lane) * 16 + (F & 1) * 8) = (u32x2){cvtpk(h[0], h[1]), cvtpk(h[2], h[3])};
    }
  }
}
__global__ __launch_bounds__(512, 2) NOPK void k_ffn1(const char* __restrict__ x1fr, const char* __restrict__ wfr, const float* __restrict__ csb, const f32x2* __restrict__ stat, char* __restrict__ hm, char* dbg) {
  extern __shared__ __attribute__((aligned(16))) char lds[];
  STAMP_DECL STAMP(0);
  const int tid = tid_x(), lane = tid & 63, wid = rfl(tid >> 6), wr = wid & 3, wc = wid >> 2;
  const int b = bid_x(), xcd = b & 7, idx = b >> 3, mt = xcd * 2 + (idx >> 4), nt = idx & 15;
  const unsigned ldsb = (unsigned)(size_t)lds;
  const __amdgpu_buffer_rsrc_t srdA = mk_srd(x1fr, 2048 * 1024), srdW = mk_srd(wfr, 16 * 16 * 14336);
  const unsigned soffA = (unsigned)((mt * 8 + wid) * 16384), soffW = (unsigned)(nt * 16 * 14336);
  f32x4 ag[4][2], au[4][2];
  if (wc) ffn1_main<1>(ldsb, lane, wid, srdA, srdW, soffA, soffW, ag, au, st_); else ffn1_main<0>(ldsb, lane, wid, srdA, srdW, soffA, soffW, ag, au, st_);
  STAMP(2);
  const int rb0 = mt * 8 + wr * 2;
  float mu[2], rs[2];
#pragma unroll
  for (int r = 0; r < 2; ++r) {
    const f32x4* sp = (const f32x4*)(stat + (size_t)((rb0 + r) * 16 + (lane & 15)) * 8);
    const f32x4 p0 = sp[0], p1 = sp[1], p2 = sp[2], p3 = sp[3];
    const float m = ((p0[0] + p0[2]) + (p1[0] + p1[2]) + (p2[0] + p2[2]) + (p3[0] + p3[2])) * 0.125f;
    const float d0 = p0[0] - m, d1 = p0[2] - m, d2 = p1[0] - m, d3 = p1[2] - m, d4 = p2[0] - m, d5 = p2[2] - m, d6 = p3[0] - m, d7 = p3[2] - m;
    const float q = ((p0[1] + p0[3]) + (p1[1] + p1[3]) + (p2[1] + p2[3]) + (p3[1] + p3[3])) + 64.f * ((d0 * d0 + d1 * d1) + (d2 * d2 + d3 * d3) + (d4 * d4 + d5 * d5) + (d6 * d6 + d7 * d7));
    mu[r] = m; rs[r] = __builtin_amdgcn_rsqf(q * (1.f / 512.f) + 1e-5f);
  }
  if (wc) ffn1_epi<1>(ag, au, csb, mu, rs, hm, nt, rb0, lane); else ffn1_epi<0>(ag, au, csb, mu, rs, hm, nt, rb0, lane);
  STAMP(3); STAMP_OUT(dbg, 4);
}
__global__ __launch_bounds__(512) NOPK void k_wprep(const u16* __restrict__ wg, const u16* __restrict__ wu, const float* __restrict__ mod, char* __restrict__ wfr, float* __restrict__ csb) {
  __shared__ float red[8][2][16];
  const int tid = tid_x(), lane = tid & 63, wid = tid >> 6, b = bid_x(), isu = b >= 112, F = isu ? b - 112 : b, c16 = lane & 15, g = lane >> 4;
  const u16* w = (isu ? wu : wg) + F * 16 + c16;
  float cs = 0.f, bs = 0.f;
#pragma unroll
  for (int i = 0; i < 2; ++i) { const int u = wid * 2 + i, k0 = u * 32 + g * 8; float o[8];
#pragma unroll
    for (int j = 0; j < 8; ++j) { const float wv = bf2f(w[(size_t)(k0 + j) * 1792]); o[j] = rbf(wv * rbf(1.f + mod[2048 + k0 + j])); cs += o[j]; bs += wv * mod[1536 + k0 + j]; }
    *(u32x4*)(wfr + ((size_t)((F / 7) * 16 + u) * 14 + isu * 7 + (F % 7)) * 1024 + lane * 16) = (u32x4){cvtpk(o[0], o[1]), cvtpk(o[2], o[3]), cvtpk(o[4], o[5]), cvtpk(o[6], o[7])}; }
  cs += shx(cs, lane, 16); cs += shx(cs, lane, 32); bs += shx(bs, lane, 16); bs += shx(bs, lane, 32);
  if (lane < 16) { red[wid][0][lane] = cs; red[wid][1][lane] = bs; }
  BARRIER();
  if (tid < 32) { float a = 0.f; for (int i = 0; i < 8; ++i) a += red[i][tid >> 4][tid & 15]; csb[isu * 3584 + (tid >> 4) * 1792 + F * 16 + (tid & 15)] = a; }
}
extern "C" void kernel_launch(void* const* d_in, const int* in_sizes, int n_in, void* d_out, int out_size, void* d_ws, size_t ws_size, hipStream_t stream) {
  const u16* x = (const u16*)d_in[0]; const u16* ckv = (const u16*)d_in[1]; const u16* wq = (const u16*)d_in[2]; const u16* wuk = (const u16*)d_in[3]; const u16* wuv = (const u16*)d_in[4]; const u16* wo = (const u16*)d_in[5];
  const u16* wg = (const u16*)d_in[7]; const u16* wu = (const u16*)d_in[8]; const u16* wd = (const u16*)d_in[9]; const u16* c = (const u16*)d_in[10]; const u16* wmod = (const u16*)d_in[11];
  char* ws = (char*)d_ws;
  float* mod = (float*)(ws + WS_MOD);
  (void)hipFuncSetAttribute((const void*)k_kv, hipFuncAttributeMaxDynamicSharedMemorySize, 131072);
  (void)hipFuncSetAttribute((const void*)k_attn, hipFuncAttributeMaxDynamicSharedMemorySize, ATT_LDS);
  (void)hipFuncSetAttribute((const void*)k_skinny<128, 0>, hipFuncAttributeMaxDynamicSharedMemorySize, SK_LDS);
  (void)hipFuncSetAttribute((const void*)k_skinny<56, 1>, hipFuncAttributeMaxDynamicSharedMemorySize, SK_LDS);
  (void)hipFuncSetAttribute((const void*)k_ffn1, hipFuncAttributeMaxDynamicSharedMemorySize, F1_LDS + 2048);
  k_mod<<<96, 256, 0, stream>>>(c, wmod, mod, ws + WS_DBG);
  k_wprep<<<224, 512, 0, stream>>>(wg, wu, mod, ws + WS_WF, (float*)(ws + WS_CSB));
  k_kv<<<256, 512, 131072, stream>>>(x, ckv, wuk, wuv, mod, ws + WS_H, ws + WS_KV, ws + WS_DBG);
  k_attn<<<256, 512, ATT_LDS, stream>>>(ws + WS_H, wq, ws + WS_KV, ws + WS_CTX, ws + WS_DBG);
  k_skinny<128, 0><<<256, 512, SK_LDS, stream>>>(ws + WS_CTX, wo, x, mod + 1024, (u16*)(ws + WS_X1), ws + WS_X1F, (f32x2*)(ws + WS_STAT), ws + WS_DBG);
  k_ffn1<<<256, 512, F1_LDS + 2048, stream>>>(ws + WS_X1F, ws + WS_WF, (const float*)(ws + WS_CSB), (const f32x2*)(ws + WS_STAT), ws + WS_HM, ws + WS_DBG);
  k_skinny<56, 1><<<256, 512, SK_LDS, stream>>>(ws + WS_HM, wd, (const u16*)(ws + WS_X1), mod + 2560, (u16*)d_out, nullptr, nullptr, ws + WS_DBG);
}
```

```cpp
#include <hip/hip_runtime.h>
#include <hip/hip_bf16.h>
typedef unsigned short u16;
typedef short bf16x8 __attribute__((ext_vector_type(8)));
typedef short s16x4 __attribute__((ext_vector_type(4)));
typedef float f32x16 __attribute__((ext_vector_type(16)));
typedef float f32x4 __attribute__((ext_vector_type(4)));
typedef float f32x2 __attribute__((ext_vector_type(2)));
typedef unsigned u32x4 __attribute__((ext_vector_type(4)));
typedef unsigned u32x2 __attribute__((ext_vector_type(2)));
typedef __bf16 bf16x2_t __attribute__((ext_vector_type(2)));
#define LAS __attribute__((address_space(3)))
#if defined(__HIP_DEVICE_COMPILE__)
#define NOPK __attribute__((target("no-packed-fp32-ops")))
#else
#define NOPK
#endif
#define DI __device__ __forceinline__ NOPK
#define MFMA16(a, b, c) __builtin_amdgcn_mfma_f32_16x16x32_bf16((a), (b), (c), 0, 0, 0)
#define SBAR() __builtin_amdgcn_sched_barrier(0)
#define WAITV(n) asm volatile("s_waitcnt vmcnt(" #n ")" ::: "memory")
#define BARRIER() asm volatile("s_waitcnt lgkmcnt(0)\n\ts_barrier" ::: "memory")
#define WAITV_BAR(n) asm volatile("s_waitcnt vmcnt(" #n ") lgkmcnt(0)\n\ts_barrier" ::: "memory")
#define WAITV_RAWBAR(n) asm volatile("s_waitcnt vmcnt(" #n ")\n\ts_barrier" ::: "memory")
#define RAWBAR() asm volatile("s_barrier" ::: "memory")

DI float bf2f(u16 b) { return __builtin_bit_cast(float, ((unsigned)b) << 16); }
DI float bflo(unsigned w) { return __builtin_bit_cast(float, w << 16); }
DI float bfhi(unsigned w) { return __builtin_bit_cast(float, w & 0xffff0000u); }
DI unsigned cvtpk(float lo, float hi) { f32x2 v = {lo, hi}; bf16x2_t b = __builtin_convertvector(v, bf16x2_t); return __builtin_bit_cast(unsigned, b); }
DI float rbf(float f) { return bflo(cvtpk(f, f) & 0xffffu); }
DI unsigned rfl(unsigned x) { return __builtin_amdgcn_readfirstlane(x); }
DI int tid_x() { return (int)__builtin_amdgcn_workitem_id_x(); }
DI int bid_x() { return (int)__builtin_amdgcn_workgroup_id_x(); }
DI float shx(float v, int lane, int m) { return __builtin_bit_cast(float, __builtin_amdgcn_ds_bpermute((lane ^ m) << 2, __builtin_bit_cast(int, v))); }
template <int CTRL> DI float dppf(float v) { return __builtin_bit_cast(float, __builtin_amdgcn_update_dpp(0, __builtin_bit_cast(int, v), CTRL, 0xf, 0xf, true)); }
DI float xl1(float v) { return dppf<0xB1>(v); }
DI float xl2(float v) { return dppf<0x4E>(v); }
DI float xm7(float v) { return dppf<0x141>(v); }
DI float xm15(float v) { return dppf<0x140>(v); }
DI float sum16x(float v) { auto r = __builtin_amdgcn_permlane16_swap(__builtin_bit_cast(unsigned, v), __builtin_bit_cast(unsigned, v), false, false); return __builtin_bit_cast(float, (unsigned)r[0]) + __builtin_bit_cast(float, (unsigned)r[1]); }
DI float sum32x(float v) { auto r = __builtin_amdgcn_permlane32_swap(__builtin_bit_cast(unsigned, v), __builtin_bit_cast(unsigned, v), false, false); return __builtin_bit_cast(float, (unsigned)r[0]) + __builtin_bit_cast(float, (unsigned)r[1]); }
DI float max16x(float v) { auto r = __builtin_amdgcn_permlane16_swap(__builtin_bit_cast(unsigned, v), __builtin_bit_cast(unsigned, v), false, false); return __builtin_fmaxf(__builtin_bit_cast(float, (unsigned)r[0]), __builtin_bit_cast(float, (unsigned)r[1])); }
DI float max32x(float v) { auto r = __builtin_amdgcn_permlane32_swap(__builtin_bit_cast(unsigned, v), __builtin_bit_cast(unsigned, v), false, false); return __builtin_fmaxf(__builtin_bit_cast(float, (unsigned)r[0]), __builtin_bit_cast(float, (unsigned)r[1])); }
DI float sum8(float v) { v += xl1(v); v += xl2(v); return v + xm7(v); }
DI float sum64(float v) { v = sum8(v); v += xm15(v); return sum32x(sum16x(v)); }
DI __amdgpu_buffer_rsrc_t mk_srd(const void* p, unsigned bytes) { return __builtin_amdgcn_make_buffer_rsrc((void*)p, (short)0, (int)bytes, 0x00020000); }
template <int IMM = 0> DI void dma16(__amdgpu_buffer_rsrc_t srd, unsigned voff, unsigned soff, unsigned ldsaddr) {
  asm volatile("s_mov_b32 m0, %0\n\ts_nop 3\n\tbuffer_load_dwordx4 %1, %2, %3 offen offset:%4 lds" ::"s"(ldsaddr), "v"(voff), "s"(srd), "s"(soff), "i"(IMM) : "memory");
}
template <int IMM = 0> DI void dma16c(__amdgpu_buffer_rsrc_t srd, unsigned voff, unsigned soff, unsigned ldsaddr) {
  asm volatile("s_mov_b32 m0, %0\n\ts_nop 3\n\tbuffer_load_dwordx4 %1, %2, %3 offen offset:%4 sc1 lds" ::"s"(ldsaddr), "v"(voff), "s"(srd), "s"(soff), "i"(IMM) : "memory");
}
DI void dma4(__amdgpu_buffer_rsrc_t srd, unsigned voff, unsigned soff, unsigned ldsaddr) {
  asm volatile("s_mov_b32 m0, %0\n\ts_nop 3\n\tbuffer_load_dword %1, %2, %3 offen lds" ::"s"(ldsaddr), "v"(voff), "s"(srd), "s"(soff) : "memory");
}
template <int IMM = 0> DI void dma16cf(__amdgpu_buffer_rsrc_t srd, unsigned voff, unsigned soff, unsigned ldsaddr) {
  asm volatile("s_mov_b32 m0, %0\n\ts_nop 0\n\tbuffer_load_dwordx4 %1, %2, %3 offen offset:%4 sc1 lds" ::"s"(ldsaddr), "v"(voff), "s"(srd), "s"(soff), "i"(IMM) : "memory");
}
template <int IMM = 0> DI void dma16cn(__amdgpu_buffer_rsrc_t srd, unsigned voff, unsigned soff) {
  asm volatile("buffer_load_dwordx4 %0, %1, %2 offen offset:%3 sc1 lds" ::"v"(voff), "s"(srd), "s"(soff), "i"(IMM) : "memory");
}
DI bf16x8 lds_rd128(unsigned addr) { return *(const LAS bf16x8*)(size_t)addr; }
typedef short v4i16_t __attribute__((ext_vector_type(4)));
DI s16x4 lds_tr(unsigned addr) { return __builtin_bit_cast(s16x4, __builtin_amdgcn_ds_read_tr16_b64_v4i16((LAS v4i16_t*)(size_t)addr)); }
DI bf16x8 cat8(s16x4 a, s16x4 b) { return __builtin_shufflevector(a, b, 0, 1, 2, 3, 4, 5, 6, 7); }
DI u32x4 pack44(const f32x4& a, const f32x4& b) { u32x4 w = {cvtpk(a[0], a[1]), cvtpk(a[2], a[3]), cvtpk(b[0], b[1]), cvtpk(b[2], b[3])}; return w; }
DI void mfma_acc(f32x4& c, const bf16x8& a, const bf16x8& b) { asm volatile("v_mfma_f32_16x16x32_bf16 %0, %1, %2, %0" : "+v"(c) : "v"(a), "v"(b)); }
DI void mfma_init(f32x4& d, const bf16x8& a, const bf16x8& b, const f32x4& c) { asm volatile("v_mfma_f32_16x16x32_bf16 %0, %1, %2, %3" : "=&v"(d) : "v"(a), "v"(b), "v"(c)); }
DI void mfma_zero(f32x4& d, const bf16x8& a, const bf16x8& b) { asm volatile("v_mfma_f32_16x16x32_bf16 %0, %1, %2, 0" : "=&v"(d) : "v"(a), "v"(b)); }
#define PIN(x) asm volatile("" : "+v"(x))
#define MFMA_DRAIN() asm volatile("s_nop 7\n\ts_nop 3" ::: "memory")
#define ST_AUX 16
DI void st16_wt(__amdgpu_buffer_rsrc_t srd, unsigned voff, unsigned soff, u32x4 v) { __builtin_amdgcn_raw_buffer_store_b128(v, srd, voff, soff, ST_AUX); }
DI u32x4 gload_asm(const void* p) { u32x4 r; asm volatile("global_load_dwordx4 %0, %1, off" : "=v"(r) : "v"(p) : "memory"); return r; }

#define CODE_ADDR(sym) ({ unsigned lo_, hi_; asm volatile("s_getpc_b64 s[96:97]\n\ts_add_u32 s96, s96, " #sym "@rel32@lo+4\n\ts_addc_u32 s97, s97, " #sym "@rel32@hi+12\n\ts_mov_b32 %0, s96\n\ts_mov_b32 %1, s97" \
    : "=s"(lo_), "=s"(hi_)::"s96", "s97", "scc"); (unsigned long long)lo_ | ((unsigned long long)hi_ << 32); })
constexpr size_t WS_MOD = 0;
constexpr size_t WS_STAT = 640 << 10;
constexpr size_t WS_SYNC = 512 << 10;
constexpr size_t WS_H = 1 << 20;
constexpr size_t WS_KV = 4 << 20;
constexpr size_t WS_CTX = 36 << 20;
constexpr size_t WS_X1 = 52 << 20;
constexpr size_t WS_HM = 54 << 20;
constexpr size_t WS_X1F = 62 << 20;
constexpr size_t WS_WT = 64 << 20;
constexpr size_t WS_CSB = 256 << 10;
constexpr size_t WS_DBG = 72 << 20;

struct Stamps {};
#define STAMP_DECL Stamps st_;
#define STAMP(i)
#define STAMP_OUT(dbg, kid)
#define TL_DECL
#define TL()
#define TL_OUT(dbg, kid)
#define ACC_DECL
#define ACC(i)
#define ACC_OUT(dbg, kid)
#define ACCX(i)
#define TLX()
#define TLD()
#define XPRIO 1
#define YPRIO 0
constexpr int FR_LDS = 163840;
constexpr float QSCALE = 0.08838834764831845f * 1.4426950408889634f;
constexpr float M_BIAS = 10.f;
#define APF_N 4
constexpr int APF = APF_N;
constexpr unsigned SPIN_CAP = 1u << 17;
DI u32x4 gload_sc1(const void* p) { u32x4 r; asm volatile("global_load_dwordx4 %0, %1, off sc1" : "=v"(r) : "v"(p) : "memory"); return r; }
DI unsigned gload1_sc1(const void* p) { unsigned r; asm volatile("global_load_dword %0, %1, off sc1" : "=v"(r) : "v"(p) : "memory"); return r; }
DI u32x2 gload2_asm(const void* p) { u32x2 r; asm volatile("global_load_dwordx2 %0, %1, off" : "=v"(r) : "v"(p) : "memory"); return r; }
DI void gstore1_sc1(void* p, unsigned v) { asm volatile("global_store_dword %0, %1, off sc1" ::"v"(p), "v"(v) : "memory"); }
DI void gstore2_sc1(void* p, u32x2 v) { asm volatile("global_store_dwordx2 %0, %1, off sc1" ::"v"(p), "v"(v) : "memory"); }
DI void gstore4_sc1(void* p, u32x4 v) { asm volatile("global_store_dwordx4 %0, %1, off sc1\n\ts_nop 1" ::"v"(p), "v"(v) : "memory"); }
DI u32x2 sload2_sc1(const void* base, unsigned off) { u32x2 r; asm volatile("s_nop 4\n\tglobal_load_dwordx2 %0, %1, %2 sc1" : "=v"(r) : "v"(off), "s"(base) : "memory"); return r; }
DI unsigned sload1(const void* base, unsigned off) { unsigned r; asm volatile("s_nop 4\n\tglobal_load_dword %0, %1, %2" : "=v"(r) : "v"(off), "s"(base) : "memory"); return r; }
DI void sstore2_sc1(void* base, unsigned off, u32x2 v) { asm volatile("s_nop 4\n\tglobal_store_dwordx2 %0, %1, %2 sc1" ::"v"(off), "v"(v), "s"(base) : "memory"); }
DI void sstore4_sc1(void* base, unsigned off, u32x4 v) { asm volatile("s_nop 4\n\tglobal_store_dwordx4 %0, %1, %2 sc1\n\ts_nop 1" ::"v"(off), "v"(v), "s"(base) : "memory"); }
DI int opaque(int v) { asm volatile("" : "+v"(v)); return v; }
DI bool all_lanes(bool c) { return __builtin_amdgcn_ballot_w64(!c) == 0; }

struct KvCtx {
  __amdgpu_buffer_rsrc_t srdA, srdB, srdW; unsigned voffA[2], voffB, soffA, soffB, ldsA, ldsB;
  unsigned aaddr[2], baddr[8];
  unsigned voffW, soffW, ldsW; int rot;
};
struct SideCtx { const char* gran; char* granw; float* modw; char* hdst; char* hflag_own; unsigned wqt, tag, ldsx, ep, cv0, cv1; u32x4 xv, xv2, wm0, wm1; int wid, lane, pub; };
DI unsigned gload1_asm(const void* p) { unsigned r; asm volatile("global_load_dword %0, %1, off" : "=v"(r) : "v"(p) : "memory"); return r; }
DI void side_partials(SideCtx& sc) {
  const int lane = opaque(sc.lane), c16 = lane & 15, g = lane >> 4;
  unsigned ep = sc.ep, cv0 = sc.cv0, cv1 = sc.cv1; u32x4 xv = sc.xv, wm0 = sc.wm0, wm1 = sc.wm1;
  asm volatile("" : "+v"(ep), "+v"(xv), "+v"(cv0), "+v"(cv1), "+v"(wm0), "+v"(wm1));
  sc.tag = rfl(ep) + 1u;
  const float c0 = bflo(cv0), c1 = bflo(cv1);
  const float s0 = rbf(c0 * __builtin_amdgcn_rcpf(1.f + __builtin_amdgcn_exp2f(-1.4426950408889634f * c0))), s1 = rbf(c1 * __builtin_amdgcn_rcpf(1.f + __builtin_amdgcn_exp2f(-1.4426950408889634f * c1)));
  float p[8];
#pragma unroll
  for (int i = 0; i < 4; ++i) { p[2 * i] = s0 * bflo(wm0[i]) + s1 * bflo(wm1[i]); p[2 * i + 1] = s0 * bfhi(wm0[i]) + s1 * bfhi(wm1[i]); }
#pragma unroll
  for (int i = 0; i < 4; ++i) { auto r = __builtin_amdgcn_permlane32_swap(__builtin_bit_cast(unsigned, p[i]), __builtin_bit_cast(unsigned, p[4 + i]), false, false); p[i] = __builtin_bit_cast(float, (unsigned)r[0]) + __builtin_bit_cast(float, (unsigned)r[1]); }
#pragma unroll
  for (int i = 0; i < 2; ++i) { auto r = __builtin_amdgcn_permlane16_swap(__builtin_bit_cast(unsigned, p[i]), __builtin_bit_cast(unsigned, p[2 + i]), false, false); p[i] = __builtin_bit_cast(float, (unsigned)r[0]) + __builtin_bit_cast(float, (unsigned)r[1]); }
#pragma unroll
  for (int i = 0; i < 2; ++i) { p[i] += xl2(p[i]); p[i] += xl1(xm7(p[i])); p[i] += xl1(xm15(p[i])); }
  if (c16 < 2) *(LAS f32x2*)(size_t)(sc.ldsx + sc.wid * 64 + (c16 * 8 + g * 2) * 4) = (f32x2){p[0], p[1]};
  float v[8], s = 0;
#pragma unroll
  for (int j = 0; j < 4; ++j) { v[2 * j] = bflo(xv[j]); v[2 * j + 1] = bfhi(xv[j]); s += v[2 * j] + v[2 * j + 1]; }
  const float mean = sum64(s) * (1.f / 512.f); float q = 0;
#pragma unroll
  for (int j = 0; j < 8; ++j) { v[j] -= mean; q += v[j] * v[j]; }
  const float rs = __builtin_amdgcn_rsqf(sum64(q) * (1.f / 512.f) + 1e-5f);
  if (lane == 0) *(LAS f32x2*)(size_t)(sc.ldsx + 512 + sc.wid * 8) = (f32x2){mean, rs};
}
template <bool ISV, int W> DI void side_hook(SideCtx& sc, __amdgpu_buffer_rsrc_t srdW, int kt) {
  if constexpr (W == 0) {
    if (kt == 2) {
      const int l2 = opaque(sc.lane); const unsigned ch = sc.wid * 8 + (l2 >> 3);
      u32x4 gr[4];
#pragma unroll
      for (int i = 0; i < 4; ++i) gr[i] = *(const LAS u32x4*)(size_t)(sc.ldsx + 4096 + (i >> 1) * 2048 + ch * 32 + (i & 1) * 16);
#pragma clang loop unroll(disable)
      for (unsigned spin = 0; spin < SPIN_CAP; ++spin) {
        if (all_lanes(gr[0][1] == sc.tag && gr[0][3] == sc.tag && gr[1][1] == sc.tag && gr[1][3] == sc.tag && gr[2][1] == sc.tag && gr[2][3] == sc.tag && gr[3][1] == sc.tag && gr[3][3] == sc.tag)) break;
        __builtin_amdgcn_s_sleep(2);
        const char* gp = sc.gran + ch * 32;
        asm volatile("global_load_dwordx4 %0, %4, off sc1\n\tglobal_load_dwordx4 %1, %4, off offset:16 sc1\n\tglobal_load_dwordx4 %2, %4, off offset:2048 sc1\n\tglobal_load_dwordx4 %3, %4, off offset:2064 sc1\n\ts_waitcnt vmcnt(0)"
                     : "=&v"(gr[0]), "=&v"(gr[1]), "=&v"(gr[2]), "=&v"(gr[3]) : "v"(gp) : "memory");
      }
      const f32x2 st = *(const LAS f32x2*)(size_t)(sc.ldsx + 512 + (l2 & 7) * 8);
      u32x4 w, xv2 = sc.xv2; asm volatile("" : "+v"(xv2));
#pragma unroll
      for (int j = 0; j < 4; ++j) { const unsigned sa = gr[j >> 1][2 * (j & 1)], ca = gr[2 + (j >> 1)][2 * (j & 1)];
        w[j] = cvtpk((bflo(xv2[j]) - st[0]) * st[1] * (1.f + bflo(ca)) + bflo(sa), (bfhi(xv2[j]) - st[0]) * st[1] * (1.f + bfhi(ca)) + bfhi(sa)); }
      sstore4_sc1(sc.hdst, ((ch >> 2) * 64 + (ch & 3) * 16 + (l2 & 7)) * 16, w);
    }
    if (kt == 4) { const int l2 = opaque(sc.lane); dma4(srdW, ((l2 >> 2) & 7) * 8192 + (l2 & 3) * 64, sc.wqt, sc.ldsx + 1024 + sc.wid * 256); }
  } else if constexpr (W == 1) {
    if (kt == 0) dma16c(mk_srd(sc.gran, 4096), opaque(sc.lane) * 16, (sc.wid & 3) * 1024, sc.ldsx + 4096 + (sc.wid & 3) * 1024);
  } else if constexpr (W == 2) {
  } else {
    if (kt == 3 && sc.wid == 0 && sc.lane == 0) gstore1_sc1(sc.hflag_own, sc.tag);
  }
}
template <int P, int BUF> DI void kv_dmaA(const KvCtx& c, int kt) { dma16(c.srdA, c.voffA[P & 1], c.soffA + P * 8192 + ((kt + c.rot) & 7) * 128, c.ldsA + BUF * 32768 + P * 1024); }
template <int P, int BUF> DI void kv_dmaB(const KvCtx& c, int kt) { dma16(c.srdB, c.voffB, c.soffB + (P >> 1) * 262144 + (P & 1) * 32768 + ((kt + c.rot) & 7) * 524288, c.ldsB + BUF * 32768 + (P >> 1) * 8192 + (P & 1) * 1024); }
#define qp_reg(J) ((J) < 2 ? 131072 + (J) * 16384 : (J) < 4 ? ((J) - 2) * 16384 : (J) < 6 ? 65536 + ((J) - 4) * 16384 : 32768 + ((J) - 6) * 16384)
template <int J, int H> DI void qp_wload1(const KvCtx& c) { dma16(c.srdW, c.voffW, c.soffW + J * 524288 + H * 32768, c.ldsW + qp_reg(J) + H * 1024); }
template <int BUF, int KS, int DT> DI bf16x8 kv_rdw(const KvCtx& c) { return cat8(lds_tr(c.baddr[DT] + BUF * 32768 + KS * 8192), lds_tr(c.baddr[DT] + BUF * 32768 + KS * 8192 + 1024)); }
template <int BUF, int KS, int KT> DI bf16x8 kv_rdc(const KvCtx& c) { return lds_rd128(c.aaddr[KS] + BUF * 32768 + KT * 2048); }
template <bool ISV, int BUF, bool LAST, int J> DI void kv_group(const KvCtx& c, SideCtx& sc, f32x4 (&acc)[8][4], bf16x8 (&wq)[4], bf16x8 (&cfA)[4], bf16x8 (&cfB)[4], int kt, u32x4 (&h0)[4], const char* hp) {
  constexpr int ks = J >> 3, dt = J & 7;
  if constexpr (J == 4 || J == 12) { WAITV_BAR(8); SBAR(); }
  const bf16x8 w = wq[J & 3];
#define KV_MM(k4) do { if (ISV) mfma_acc(acc[dt][k4], ks ? cfB[k4] : cfA[k4], w); else mfma_acc(acc[dt][k4], w, ks ? cfB[k4] : cfA[k4]); } while (0)
  KV_MM(0);
  bf16x8 wn = w;
  if constexpr (J + 4 < 16) wn = kv_rdw<BUF, ((J + 4) >> 3), ((J + 4) & 7)>(c); else if constexpr (!(LAST && BUF == 1)) wn = kv_rdw<BUF ^ 1, 0, J + 4 - 16>(c);
  SBAR();
  KV_MM(1);
  if constexpr (J < 4) cfB[J] = kv_rdc<BUF, 1, J>(c);
  if constexpr (J >= 12 && !(LAST && BUF == 1)) cfA[J - 12] = kv_rdc<BUF ^ 1, 0, J - 12>(c);
  SBAR();
  KV_MM(2);
  if constexpr (J >= 4 && J < 10) {
    if constexpr (!LAST) { if constexpr (J < 8) kv_dmaA<J - 4, BUF>(c, kt + 2); else kv_dmaB<J - 8, BUF>(c, kt + 2); }
    else if constexpr (BUF == 0) qp_wload1<(J - 4) / 2, (J - 4) & 1>(c);
    else qp_wload1<(J < 6 ? 5 : J < 8 ? 6 : 7), J & 1>(c);
  }
  if constexpr (J == 12 || J == 13) {
    if constexpr (!LAST) kv_dmaB<J - 10, BUF>(c, kt + 2);
    else if constexpr (BUF == 0) qp_wload1<3, J & 1>(c);
  }
  if constexpr ((J == 14 || J == 15) && LAST && BUF == 0) qp_wload1<4, J & 1>(c);
  if constexpr (J >= 10 && J < 14 && LAST && BUF == 1) h0[J - 10] = gload_sc1(hp + ((J - 10) & 1) * 16384 + ((J - 10) >> 1) * 1024);
  SBAR();
  KV_MM(3);
#undef KV_MM
  wq[J & 3] = wn;
  if constexpr (!LAST && J == 10) { SBAR(); side_hook<ISV, 2 * BUF>(sc, c.srdW, kt); }
  if constexpr (!LAST && J == 14) { SBAR(); side_hook<ISV, 2 * BUF + 1>(sc, c.srdW, kt); }
  SBAR();
}
template <bool ISV, int BUF, bool LAST> DI void kv_tile(const KvCtx& c, SideCtx& sc, f32x4 (&acc)[8][4], bf16x8 (&wq)[4], bf16x8 (&cfA)[4], bf16x8 (&cfB)[4], int kt, u32x4 (&h0)[4], const char* hp) {
#define G(J) kv_group<ISV, BUF, LAST, J>(c, sc, acc, wq, cfA, cfB, kt, h0, hp);
  G(0) G(1) G(2) G(3) G(4) G(5) G(6) G(7) G(8) G(9) G(10) G(11) G(12) G(13) G(14) G(15)
#undef G
}
template <bool ISV> DI void kv_main(KvCtx& c, SideCtx& sc, u32x4 (&pk)[16], const char* hflag_need, u32x4 (&h0)[4], const char* hp, char* dbg) {
  f32x4 acc[8][4];
#pragma unroll
  for (int dt = 0; dt < 8; ++dt)
#pragma unroll
    for (int kt = 0; kt < 4; ++kt) { acc[dt][kt] = f32x4{}; PIN(acc[dt][kt]); }
  bf16x8 wq[4], cfA[4], cfB[4];
  cfA[0] = kv_rdc<0, 0, 0>(c); cfA[1] = kv_rdc<0, 0, 1>(c); cfA[2] = kv_rdc<0, 0, 2>(c); cfA[3] = kv_rdc<0, 0, 3>(c);
  wq[0] = kv_rdw<0, 0, 0>(c); wq[1] = kv_rdw<0, 0, 1>(c); wq[2] = kv_rdw<0, 0, 2>(c); wq[3] = kv_rdw<0, 0, 3>(c);
  SBAR();
  TL_DECL
#pragma clang loop unroll(disable)
  for (int it = 0; it < 3; ++it) {
    TLX();
    kv_tile<ISV, 0, false>(c, sc, acc, wq, cfA, cfB, 2 * it, h0, hp);
    TLX();
    kv_tile<ISV, 1, false>(c, sc, acc, wq, cfA, cfB, 2 * it + 1, h0, hp);
  }
  TLX();
  u32x4 hfl = gload_sc1(hflag_need);
  kv_tile<ISV, 0, true>(c, sc, acc, wq, cfA, cfB, 6, h0, hp);
  TLX();
  asm volatile("s_waitcnt vmcnt(10)" : "+v"(hfl)::"memory");
#pragma clang loop unroll(disable)
  for (unsigned spin = 0; spin < SPIN_CAP; ++spin) {
    if (all_lanes(hfl[0] == sc.tag && hfl[1] == sc.tag && hfl[2] == sc.tag && hfl[3] == sc.tag)) break;
    __builtin_amdgcn_s_sleep(2);
    asm volatile("global_load_dwordx4 %0, %1, off sc1\n\ts_waitcnt vmcnt(0)" : "=&v"(hfl) : "v"(hflag_need) : "memory");
  }
  SBAR();
  kv_tile<ISV, 1, true>(c, sc, acc, wq, cfA, cfB, 7, h0, hp);
  TLX();
  MFMA_DRAIN();
  if constexpr (ISV) {
#pragma unroll
    for (int kp = 0; kp < 2; ++kp)
#pragma unroll
      for (int dt = 0; dt < 8; ++dt) pk[8 * kp + dt] = pack44(acc[dt][2 * kp], acc[dt][2 * kp + 1]);
  } else {
#pragma unroll
    for (int ds = 0; ds < 4; ++ds)
#pragma unroll
      for (int kt = 0; kt < 4; ++kt) pk[4 * ds + kt] = pack44(acc[2 * ds][kt], acc[2 * ds + 1][kt]);
  }
#pragma unroll
  for (int i = 0; i < 16; ++i) PIN(pk[i]);
  asm volatile("s_waitcnt vmcnt(0)" : "+v"(h0[0]), "+v"(h0[1]), "+v"(h0[2]), "+v"(h0[3])::"memory");
}

struct AttState {
  f32x4 o[8][2], s[4][2], negm[2], l[2];
  u32x4 p[2][2];
  bf16x8 q[2][4];
  bf16x8 f[APF], ones;
  float m[2];
};
DI void att_qk(AttState& a, unsigned kb, unsigned vb) {
#pragma unroll
  for (int mm = 0; mm < 16; ++mm) {
    const int ds = mm >> 2, kt = mm & 3;
    const bf16x8 fr = a.f[mm % APF];
    if (ds == 0) { mfma_init(a.s[kt][0], fr, a.q[0][0], a.negm[0]); mfma_init(a.s[kt][1], fr, a.q[1][0], a.negm[1]); }
    else { mfma_acc(a.s[kt][0], fr, a.q[0][ds]); mfma_acc(a.s[kt][1], fr, a.q[1][ds]); }
    const int n = mm + APF;
    if (n < 16) a.f[mm % APF] = lds_rd128(kb + n * 1024); else a.f[mm % APF] = lds_rd128(vb + (n - 16) * 1024);
    SBAR();
  }
}
template <bool PRE> DI void att_pv(AttState& a, unsigned vb) {
  if constexpr (!PRE) {
#pragma unroll
    for (int i = 0; i < APF; ++i) a.f[i] = lds_rd128(vb + i * 1024);
    SBAR();
  }
#pragma unroll
  for (int mm = 0; mm < 16; ++mm) {
    const int kp = mm >> 3, dt = mm & 7;
    const bf16x8 fr = a.f[mm % APF];
    mfma_acc(a.o[dt][0], fr, __builtin_bit_cast(bf16x8, a.p[kp][0]));
    mfma_acc(a.o[dt][1], fr, __builtin_bit_cast(bf16x8, a.p[kp][1]));
    if (mm >= 12) mfma_acc(a.l[mm & 1], a.ones, __builtin_bit_cast(bf16x8, a.p[(mm >> 1) & 1][mm & 1]));
    const int n = mm + APF;
    if (n < 16) a.f[mm % APF] = lds_rd128(vb + n * 1024);
    SBAR();
  }
}
DI float red_g_max(float v, int lane) { return max32x(max16x(v)); }
DI float red_g_sum(float v, int lane) { return sum32x(sum16x(v)); }
DI void att_store(const AttState& a, const float (&sc)[2], char* dst  , unsigned voffL) {
  const __amdgpu_buffer_rsrc_t srd = mk_srd(dst, 128 * 1024 + 4096);
#pragma unroll
  for (int qt = 0; qt < 2; ++qt)
#pragma unroll
    for (int ds = 0; ds < 4; ++ds) st16_wt(srd, voffL + qt * (128 * 1024) + ds * 1024, 0, pack44(a.o[2 * ds][qt] * sc[qt], a.o[2 * ds + 1][qt] * sc[qt]));
}

DI void rt_load(const u16* wo, const u16* wd, int wi, int lane, unsigned stg) {
  const int isd = wi >= 1024, bi = isd ? wi - 1024 : wi, u = bi >> 3, P = u / 7, j = u - P * 7, T0 = isd ? 14 * P + (j <= 3 ? 2 * j : 2 * j - 1) : 2 * u;
  const __amdgpu_buffer_rsrc_t srdT = mk_srd(isd ? wd : wo, isd ? 1792 * 1024 : 4096 * 1024);
  const unsigned vo = (unsigned)((lane >> 3) * 1024 + (((lane & 7) ^ (((lane >> 4) & 3) << 1)) * 16)), so = (unsigned)(T0 * 16384 + (bi & 7) * 128), sx = (isd && j == 3) ? 6 * 16384 : 0;
  dma16(srdT, vo, so, stg); dma16(srdT, vo, so + 8192, stg + 1024); dma16(srdT, vo, so + 16384 + sx, stg + 2048); dma16(srdT, vo, so + 24576 + sx, stg + 3072);
}
DI void rt_store(char* ws, int wi, int lane, unsigned stg) {
  const int isd = wi >= 1024, bi = isd ? wi - 1024 : wi, cc = lane & 15, gg = lane >> 4, q = cc >> 2, p = cc & 3, f = (2 * gg + (q >> 1)) & 3;
  const __amdgpu_buffer_rsrc_t srdD = mk_srd(ws + WS_WT + (isd ? (4 << 20) : 0) + (size_t)((bi & 7) * (isd ? 56 : 128) + (bi >> 3)) * 4096, wi < 1472 ? 4096 : 0);
#pragma unroll
  for (int ct = 0; ct < 4; ++ct) { const unsigned ad = stg + (4 * gg + q) * 128 + ((ct ^ f) << 5) + 8 * p;
    st16_wt(srdD, lane * 16 + ct * 1024, 0, __builtin_bit_cast(u32x4, cat8(lds_tr(ad), lds_tr(ad + 2048)))); }
}
#define POLL_SLEEP 4
DI void poll_words(const char* p, unsigned tag) {
#pragma clang loop unroll(disable)
  for (unsigned spin = 0; spin < SPIN_CAP; ++spin) {
    unsigned f;
    asm volatile("global_load_dword %0, %1, off sc1\n\ts_waitcnt vmcnt(0)" : "=&v"(f) : "v"(p) : "memory");
    if (all_lanes(f == tag)) break;
    __builtin_amdgcn_s_sleep(POLL_SLEEP);
  }
}
#define SKD 4
DI void bload4(u32x4& d, __amdgpu_buffer_rsrc_t srd, unsigned voff, unsigned soff) { asm volatile("s_nop 4\n\tbuffer_load_dwordx4 %0, %1, %2, %3 offen sc1" : "=v"(d) : "v"(voff), "s"(srd), "s"(soff) : "memory"); }
DI void bload2(u32x2& d, __amdgpu_buffer_rsrc_t srd, unsigned voff, unsigned soff) { asm volatile("s_nop 4\n\tbuffer_load_dwordx2 %0, %1, %2, %3 offen sc1" : "=v"(d) : "v"(voff), "s"(srd), "s"(soff) : "memory"); }
template <int NU, class Extra> DI void skinny_main(const char* __restrict__ afr, const char* __restrict__ wt, unsigned ldsb, unsigned ring, int rt, int ct0, int tid, int wid, const char* flagp, unsigned tag,
                                                   int mode, char* ownflag, Extra extra, f32x4& s0, f32x4& s1, Stamps& st_) {
  constexpr int NS = NU / 8;
  const int lane = tid & 63, c16 = lane & 15, g = lane >> 4;
  const __amdgpu_buffer_rsrc_t srdW = mk_srd(wt, NU * 32 * 1024), srdA = mk_srd(afr, 128 * NU * 1024);
  const unsigned voffL = lane * 16, voffH = lane * 8;
  const unsigned soffW = (unsigned)((ct0 * NU + wid * NS) * 4096), soffA = (unsigned)((rt * 4 * NU + wid * NS) * 1024);
  u32x4 fr[SKD][8]; u32x2 fh[8];
#define OP_LD(st, i) do { if ((i) < 4) { if (NU == 56 && (st) == 3) { bload2(fh[(2 * (i)) & 7], srdA, voffH, soffA + (i) * (NU * 1024) + (st) * 1024); bload2(fh[(2 * (i) + 1) & 7], srdA, voffH, soffA + (i) * (NU * 1024) + (st) * 1024 + 512); } \
      else bload4(fr[(st) % SKD][i], srdA, voffL, soffA + (i) * (NU * 1024) + (st) * 1024); } \
    else bload4(fr[(st) % SKD][i], srdW, voffL, soffW + (st) * 4096 + ((i) - 4) * 1024); } while (0)
#pragma unroll
  for (int st = 0; st < SKD - 1; ++st) { OP_LD(st, 4); OP_LD(st, 5); OP_LD(st, 6); OP_LD(st, 7); }
  if (mode == 0) { RAWBAR(); poll_words(flagp, tag); }
  else { asm volatile("s_waitcnt vmcnt(%0)" ::"n"(4 * (SKD - 1)) : "memory"); RAWBAR(); if (tid == 0) gstore1_sc1(ownflag, tag); if (wid == 0) poll_words(flagp, tag); RAWBAR(); }
  STAMP(1);
#pragma unroll
  for (int st = 0; st < SKD - 1; ++st) { OP_LD(st, 0); OP_LD(st, 1); OP_LD(st, 2); OP_LD(st, 3); }
  f32x4 acc[4][4];
#pragma unroll
  for (int st = 0; st < NS; ++st) {
    constexpr int SD = SKD - 1;
    const int young = st < SD ? 4 * (SD - 1 - st) + 8 * (st < NS - SD ? st : NS - SD) : 8 * ((st + SD - 1 < NS ? st + SD - 1 : NS - 1) - st);
    u32x4* f = fr[st % SKD];
    if (NU == 56 && st == 3) asm volatile("s_waitcnt vmcnt(%12)" : "+v"(fh[0]), "+v"(fh[1]), "+v"(fh[2]), "+v"(fh[3]), "+v"(fh[4]), "+v"(fh[5]), "+v"(fh[6]), "+v"(fh[7]), "+v"(f[4]), "+v"(f[5]), "+v"(f[6]), "+v"(f[7]) : "n"(young) : "memory");
    else asm volatile("s_waitcnt vmcnt(%8)" : "+v"(f[0]), "+v"(f[1]), "+v"(f[2]), "+v"(f[3]), "+v"(f[4]), "+v"(f[5]), "+v"(f[6]), "+v"(f[7]) : "n"(young) : "memory");
    if (st == 0) STAMP(2);
    if (NU == 56 && st == 3) {
#pragma unroll
      for (int i = 0; i < 4; ++i) { f[i] = (u32x4){fh[2 * i][0], fh[2 * i][1], fh[2 * i + 1][0], fh[2 * i + 1][1]}; PIN(f[i]); }
      asm volatile("s_nop 1");
    }
#pragma unroll
    for (int m = 0; m < 16; ++m) {
      const int ct = m >> 2, r = m & 3;
      if (st == 0) mfma_zero(acc[ct][r], __builtin_bit_cast(bf16x8, f[4 + ct]), __builtin_bit_cast(bf16x8, f[r])); else mfma_acc(acc[ct][r], __builtin_bit_cast(bf16x8, f[4 + ct]), __builtin_bit_cast(bf16x8, f[r]));
      if (st + SD < NS) { if (!(m & 1)) OP_LD(st + SD, m >> 1); }
      else if (st == NS - 1) extra(m);
      SBAR();
    }
  }
#undef OP_LD
  MFMA_DRAIN();
  STAMP(3);
#pragma unroll
  for (int ct = 0; ct < 4; ++ct)
#pragma unroll
    for (int r = 0; r < 4; ++r) *(LAS f32x4*)(size_t)(ldsb + wid * (64 * 272) + (r * 16 + c16) * 272 + (ct * 16 + 4 * g) * 4) = acc[ct][r];
  WAITV_BAR(0);
  const int row = tid >> 3, cg = tid & 7;
  s0 = f32x4{}; s1 = f32x4{};
#pragma unroll
  for (int wv = 0; wv < 8; ++wv) { s0 += *(const LAS f32x4*)(size_t)(ldsb + wv * (64 * 272) + row * 272 + cg * 32); s1 += *(const LAS f32x4*)(size_t)(ldsb + wv * (64 * 272) + row * 272 + cg * 32 + 16); }
}
DI u32x4 resid8(const u32x4& rv, const u32x4& g0, const u32x4& g1, const f32x4& s0, const f32x4& s1) {
  return (u32x4){cvtpk(bflo(rv[0]) + bflo(g0[0]) * s0[0], bfhi(rv[0]) + bfhi(g0[0]) * s0[1]), cvtpk(bflo(rv[1]) + bflo(g0[2]) * s0[2], bfhi(rv[1]) + bfhi(g0[2]) * s0[3]),
                 cvtpk(bflo(rv[2]) + bflo(g1[0]) * s1[0], bfhi(rv[2]) + bfhi(g1[0]) * s1[1]), cvtpk(bflo(rv[3]) + bflo(g1[2]) * s1[2], bfhi(rv[3]) + bfhi(g1[2]) * s1[3])};
}

constexpr int F1_STAGE = 22 * 1024, F1_NST = 7, F1_LDS = F1_STAGE * F1_NST;

DI void tail_phases(const u16* __restrict__ x, const u16* __restrict__ wg, const u16* __restrict__ wu, u16* __restrict__ out, char* __restrict__ ws, unsigned ldsb, unsigned tag, bool kvok) {
  char* const dbg = ws + WS_DBG; (void)dbg;
  const int tid = opaque(tid_x()), lane = tid & 63, wid = rfl(tid >> 6);
  const int b = bid_x(), xcd = b & 7, idx = b >> 3, mtp = (idx >> 4) * 8 + xcd, rt = mtp * 2 + ((idx >> 3) & 1), ct0 = idx & 7;
  const int c16 = lane & 15, g = lane >> 4;
  char* const sync = ws + WS_SYNC;
  const int row = tid >> 3, cg = tid & 7, R = rt * 64 + row, C = ct0 * 64 + cg * 8;
  const unsigned voffL = lane * 16;
  u32x4 x1v;
  {
    STAMP_DECL STAMP(0);
#pragma clang loop unroll(disable)
    for (unsigned spin = 0; !kvok && spin < SPIN_CAP; ++spin) {
      u32x4 f;
      asm volatile("global_load_dwordx4 %0, %1, off sc1\n\ts_waitcnt vmcnt(0)" : "=&v"(f) : "v"(sync + 3072 + lane * 16) : "memory");
      if (all_lanes(f[0] == tag && f[1] == tag && f[2] == tag && f[3] == tag)) break;
      __builtin_amdgcn_s_sleep(1);
    }
    u32x4 rv, gv[4]; f32x4 s0, s1;
    skinny_main<128>(ws + WS_CTX, ws + WS_WT, ldsb, ldsb + (wid & 3) * 32768 + (wid < 4 ? 16384 : 0), rt, ct0, tid, wid,
                     sync + 16384 + ((rt * 2 + ((lane >> 2) & 1)) * 32 + wid * 4 + (lane & 3)) * 4, tag, 0, nullptr,
                     [&](int m) NOPK { if (m == 0) rv = gload_asm(x + (size_t)R * 512 + C);
                       if (m == 2) gv[0] = gload_sc1(sync + 4096 + (1024 + C) * 4); if (m == 4) gv[1] = gload_sc1(sync + 4096 + (1024 + C) * 4 + 16);
                       if (m == 6) gv[2] = gload_sc1(sync + 4096 + (2048 + C) * 4); if (m == 8) gv[3] = gload_sc1(sync + 4096 + (2048 + C) * 4 + 16); }, s0, s1, st_);
    asm volatile("" : "+v"(rv), "+v"(gv[0]), "+v"(gv[1]), "+v"(gv[2]), "+v"(gv[3]));
    x1v = resid8(rv, gv[0], gv[1], s0, s1);
    float v[8];
#pragma unroll
    for (int j = 0; j < 4; ++j) { v[2 * j] = bflo(x1v[j]); v[2 * j + 1] = bfhi(x1v[j]); }
    const unsigned stg = ldsb + 139264, fp = stg + ((row >> 4) * 2 + (cg >> 2)) * 1024 + (row & 15) * 16 + ((cg >> 1) & 1) * 8 + (cg & 1) * 512;
    *(LAS u32x2*)(size_t)fp = (u32x2){cvtpk(v[0] * rbf(1.f + bflo(gv[2][0])), v[1] * rbf(1.f + bfhi(gv[2][0]))), cvtpk(v[2] * rbf(1.f + bflo(gv[2][2])), v[3] * rbf(1.f + bfhi(gv[2][2])))};
    *(LAS u32x2*)(size_t)(fp + 256) = (u32x2){cvtpk(v[4] * rbf(1.f + bflo(gv[3][0])), v[5] * rbf(1.f + bfhi(gv[3][0]))), cvtpk(v[6] * rbf(1.f + bflo(gv[3][2])), v[7] * rbf(1.f + bfhi(gv[3][2])))};
    float s = ((v[0] + v[1]) + (v[2] + v[3])) + ((v[4] + v[5]) + (v[6] + v[7]));
    s += xl1(s); s += xl2(s);
    const float mean = s * (1.f / 32.f); float q = 0.f;
#pragma unroll
    for (int j = 0; j < 8; ++j) { const float d = v[j] - mean; q += d * d; }
    q += xl1(q); q += xl2(q);
    if ((cg & 3) == 0) *(LAS f32x2*)(size_t)(stg + 8192 + row * 16 + (cg >> 2) * 8) = (f32x2){mean, q};
    BARRIER();
    st16_wt(mk_srd(ws + WS_X1F + (size_t)((rt * 4 + (wid >> 1)) * 16 + ct0 * 2 + (wid & 1)) * 1024, 1024), voffL, 0, *(const LAS u32x4*)(size_t)(stg + wid * 1024 + lane * 16));
    if (wid == 0) st16_wt(mk_srd(ws + WS_STAT + (rt * 8 + ct0) * 1024, 1024), voffL, 0, *(const LAS u32x4*)(size_t)(stg + 8192 + lane * 16));
    STAMP(4); STAMP_OUT(dbg, 3);
  }
  {
    STAMP_DECL STAMP(0);
    const int mt = mtp, nt = idx & 15, wr = wid & 3, wc = wid >> 2, F0 = wc * 4, rb0 = mt * 8 + wr * 2;
    const bool four = wid < 4;
    const __amdgpu_buffer_rsrc_t srdA = mk_srd(ws + WS_X1F, 2048 * 1024), srd1 = mk_srd(wid < 7 ? wg : wu, 512 * 1792 * 2), srd2 = mk_srd(wu, 512 * 1792 * 2);
    unsigned voff1, voff2;
    { const int i1 = (wid < 7 ? wid : 0) * 64 + lane, r1 = i1 / 14, i2 = (wid + 1) * 64 + lane, r2 = i2 / 14;
      voff1 = r1 * 3584 + (i1 - r1 * 14) * 16; voff2 = r2 * 3584 + (i2 - r2 * 14) * 16; }
    const unsigned soffA = (unsigned)((mt * 8 + wid) * 16384), soffW = (unsigned)(nt * 224), soffW2 = wid < 6 ? soffW : 0x7f000000u;
    const unsigned d1 = ldsb + 8192 + wid * 1024, d2 = wid < 6 ? ldsb + 16384 + wid * 1024 : ldsb + F1_LDS + (wid - 6) * 1024, d2step = wid < 6 ? F1_STAGE : 0;
#define F1_LA(u) dma16c(srdA, voffL, soffA + (u) * 1024, ldsb + ((u) % F1_NST) * F1_STAGE + wid * 1024)
#define F1_LW1(u) dma16(srd1, voff1, soffW + (u) * 114688, d1 + ((u) % F1_NST) * F1_STAGE)
#define F1_LW2(u) dma16(srd2, voff2, soffW2 + (u) * 114688, d2 + ((u) % F1_NST) * d2step)
#pragma unroll
    for (int u = 0; u < 3; ++u) { F1_LW1(u); F1_LW2(u); }
    WAITV(6); RAWBAR();
    if (tid == 0) gstore1_sc1(sync + 24576 + (rt * 8 + ct0) * 4, tag);
#pragma unroll
    for (int u = 3; u < 6; ++u) { F1_LW1(u); F1_LW2(u); }
    STAMP(1);
    if (wid == 0) poll_words(sync + 24576 + (mt * 16 + (lane & 15)) * 4, tag);
    RAWBAR();
    STAMP(2);
#pragma unroll
    for (int u = 0; u < 6; ++u) F1_LA(u);
    const unsigned rdA = ldsb + wr * 2048 + lane * 16, rdW = ldsb + 8192 + (4 * g + (c16 >> 2)) * 224 + F0 * 32 + 8 * (c16 & 3);
    unsigned bW[4], bA[4];
#pragma unroll
    for (int k = 0; k < 4; ++k) { bW[k] = rdW + 2 * k * F1_STAGE; bA[k] = rdA + 2 * k * F1_STAGE; PIN(bW[k]); PIN(bA[k]); }
    bf16x8 xa[2][2]; s16x4 wl[2][8], wh[2][8];
    f32x4 ag[4][2], au[4][2];
    u32x4 sp[2][2], csv[2];
    float mu[2], rs[2];
    const unsigned cst = ldsb + F1_LDS + 2048;
    const char* statp = ws + WS_STAT + ((rb0 >> 2) * 8 + g * 2) * 1024 + ((rb0 & 3) * 16 + c16) * 16;
    const char* csp = ws + WS_CSB + (tid < 112 ? ((tid / 28) * 1792 + nt * 112 + (tid % 28) * 4) * 8 : 0);
#define F1_SL(u) ((u) % F1_NST)
#define F1_TR(u, i, h) lds_tr(bW[F1_SL(u) >> 1] + (F1_SL(u) & 1) * F1_STAGE + ((i) >> 2) * 7168 + ((i) & 3) * 32 + (h) * 3584)
#define F1_XA(u, r) lds_rd128(bA[F1_SL(u) >> 1] + (F1_SL(u) & 1) * F1_STAGE + (r) * 1024)
    WAITV_BAR(5);
    STAMP(3);
    xa[0][0] = F1_XA(0, 0); xa[0][1] = F1_XA(0, 1);
#pragma unroll
    for (int i = 0; i < 8; ++i) { wl[0][i] = F1_TR(0, i, 0); wh[0][i] = F1_TR(0, i, 1); }
    SBAR();
#pragma unroll
    for (int u = 0; u < 16; ++u) {
      const int cu = u & 1, nx = cu ^ 1;
      if (u == 0) WAITV_RAWBAR(3); else if (u == 2) WAITV_RAWBAR(4); else if (u == 4 || u == 6 || u == 8 || u == 10) WAITV_RAWBAR(6); else if (u == 12) WAITV_RAWBAR(3); else if (u == 14) WAITV_RAWBAR(0);
      SBAR();
#pragma unroll
      for (int m = 0; m < 16; ++m) {
        const int f = m >> 2, isu = (m >> 1) & 1, r = m & 1, i = isu * 4 + f;
        if (u == 0 || f < 3 || four) {
          const bf16x8 w = cat8(wl[cu][i], wh[cu][i]);
          f32x4& d = isu ? au[f][r] : ag[f][r];
          if (u == 0) mfma_zero(d, w, xa[cu][r]); else mfma_acc(d, w, xa[cu][r]);
        }
        if (u + 1 < 16) {
          if (m == 0) { xa[nx][0] = F1_XA(u + 1, 0); xa[nx][1] = F1_XA(u + 1, 1); }
          else if (m <= 8) { const int j = m - 1, i2 = (j & 1) * 4 + (j >> 1); wl[nx][i2] = F1_TR(u + 1, i2, 0); wh[nx][i2] = F1_TR(u + 1, i2, 1); }
        }
        if (u >= 1 && u + 5 < 16) { if (m == 2) F1_LA(u + 5); if (m == 6) F1_LW1(u + 5); if (m == 10) F1_LW2(u + 5); }
        if (m == 11 && u >= 4 && u < 10) { if (u < 8) sp[(u - 4) >> 1][u & 1] = gload_sc1(statp + ((u - 4) >> 1) * 256 + (u & 1) * 1024); else csv[u - 8] = gload_sc1(csp + (u - 8) * 16); }
        if (m < 12 || m == 15) SBAR();
      }
      if (u == 12) {
    asm volatile("" : "+v"(sp[0][0]), "+v"(sp[0][1]), "+v"(sp[1][0]), "+v"(sp[1][1]), "+v"(csv[0]), "+v"(csv[1]));
#pragma clang loop unroll(disable)
    for (unsigned spin = 0; spin < SPIN_CAP; ++spin) {
      if (all_lanes(csv[0][1] == tag && csv[0][3] == tag && csv[1][1] == tag && csv[1][3] == tag)) break;
      __builtin_amdgcn_s_sleep(1);
      asm volatile("global_load_dwordx4 %0, %2, off sc1\n\tglobal_load_dwordx4 %1, %2, off offset:16 sc1\n\ts_waitcnt vmcnt(0)" : "=&v"(csv[0]), "=&v"(csv[1]) : "v"(csp) : "memory");
    }
    if (tid < 112) *(LAS u32x4*)(size_t)(cst + tid * 16) = (u32x4){csv[0][0], csv[0][2], csv[1][0], csv[1][2]};
#pragma unroll
    for (int r = 0; r < 2; ++r) {
      const f32x4 p0 = __builtin_bit_cast(f32x4, sp[r][0]), p1 = __builtin_bit_cast(f32x4, sp[r][1]);
      const float m = sum32x(sum16x((p0[0] + p0[2]) + (p1[0] + p1[2]))) * 0.0625f, d0 = p0[0] - m, d1 = p0[2] - m, d2_ = p1[0] - m, d3 = p1[2] - m;
      const float q = sum32x(sum16x(((p0[1] + p0[3]) + (p1[1] + p1[3])) + 32.f * ((d0 * d0 + d1 * d1) + (d2_ * d2_ + d3 * d3))));
      mu[r] = m; rs[r] = __builtin_amdgcn_rsqf(q * (1.f / 512.f) + 1e-5f); PIN(mu[r]); PIN(rs[r]);
    }
    asm volatile("s_waitcnt lgkmcnt(0)" ::: "memory");
      SBAR(); }
    }
#undef F1_TR
#undef F1_XA
#undef F1_SL
#undef F1_LA
#undef F1_LW1
#undef F1_LW2
    MFMA_DRAIN();
    STAMP(4);
    const unsigned csl = cst + g * 16 + F0 * 64;
    u32x2 hv[4][2];
#pragma unroll
    for (int f = 0; f < 4; ++f) {
      const unsigned a = csl + f * 64;
      const f32x4 csg = *(const LAS f32x4*)(size_t)(a), bg = *(const LAS f32x4*)(size_t)(a + 448), csu = *(const LAS f32x4*)(size_t)(a + 896), bu = *(const LAS f32x4*)(size_t)(a + 1344);
#pragma unroll
      for (int r = 0; r < 2; ++r) {
        float h[4];
#pragma unroll
        for (int j = 0; j < 4; ++j) { const float gv = rs[r] * (ag[f][r][j] - mu[r] * csg[j]) + bg[j], uv = rs[r] * (au[f][r][j] - mu[r] * csu[j]) + bu[j]; h[j] = gv > 0.f ? gv * uv : 0.f; }
        hv[f][r] = (u32x2){cvtpk(h[0], h[1]), cvtpk(h[2], h[3])};
      }
    }
    const __amdgpu_buffer_rsrc_t srdH = mk_srd(ws + WS_HM + (size_t)(rb0 * 56 + (nt >> 1) * 7) * 1024, 64 * 1024);
    const unsigned j0 = ((nt & 1) * 4 + wc * 2) * 1024;
#pragma unroll
    for (int r = 0; r < 2; ++r) {
      st16_wt(srdH, voffL + j0 + r * 57344, 0, (u32x4){hv[0][r][0], hv[0][r][1], hv[1][r][0], hv[1][r][1]});
      if (four) st16_wt(srdH, voffL + j0 + 1024 + r * 57344, 0, (u32x4){hv[2][r][0], hv[2][r][1], hv[3][r][0], hv[3][r][1]});
      else __builtin_amdgcn_raw_buffer_store_b64(hv[2][r], srdH, lane * 8 + 3072 + (nt & 1) * 512 + r * 57344, 0, ST_AUX);
    }
    STAMP(5); STAMP_OUT(dbg, 4);
  }
  {
    STAMP_DECL STAMP(0);
    u32x4 gv[2]; f32x4 s0, s1;
    skinny_main<56>(ws + WS_HM, ws + WS_WT + (4 << 20), ldsb, ldsb + wid * 16384, rt, ct0, tid, wid,
                    sync + 25600 + (rt >> 1) * 128 + (lane & 15) * 4, tag, 1, sync + 25600 + mtp * 128 + (idx & 15) * 4,
                    [&](int m) NOPK { if (m == 0) gv[0] = gload_sc1(sync + 4096 + (2560 + C) * 4); if (m == 2) gv[1] = gload_sc1(sync + 4096 + (2560 + C) * 4 + 16); }, s0, s1, st_);
    asm volatile("" : "+v"(gv[0]), "+v"(gv[1]));
    *(u32x4*)(out + (size_t)R * 512 + C) = resid8(x1v, gv[0], gv[1], s0, s1);
    STAMP(4); STAMP_OUT(dbg, 5);
  }
}
struct FrontArgs { const u16 *x, *ckv, *wq, *wuk, *wuv, *c, *wmod, *pf0, *pf1, *pf2, *pf3, *out; char* ws; };
extern "C" __global__ __launch_bounds__(512, 2) NOPK void k_front(FrontArgs A) {
  extern __shared__ __attribute__((aligned(16))) char lds[];
  STAMP_DECL STAMP(0);
  char* const ws = A.ws; char* const dbg = ws + WS_DBG; (void)dbg;
  const int tid = tid_x(), lane = tid & 63, wid = rfl(tid >> 6), grp = wid >> 2, w4 = wid & 3;
  const int b = bid_x(), xcd = b & 7, idx = b >> 3, head = xcd * 4 + (idx >> 3), pi = idx & 7;
  const int c16 = lane & 15, g = lane >> 4;
  const unsigned ldsb = (unsigned)(size_t)lds;
  const int blkA = pi, blkB = 15 - pi, nA = 2 * pi + 2;
  char* const sync = ws + WS_SYNC;
  unsigned tag;
  u32x4 pk[16], h0[4];
  KvCtx c;
  const __amdgpu_buffer_rsrc_t srdO = mk_srd(ws + WS_KV + (size_t)head * (1 << 20) + (pi * 4 + w4) * 32768 + grp * 16384, 16384);
  const int rb0 = ((grp ? blkB : blkA) * 128 + w4 * 32) >> 4;
  const char* hp = ws + WS_H + (size_t)rb0 * 16384 + lane * 16;
  {
    const int cb = idx < 24 ? xcd * 24 + idx : 192 + idx;
    SideCtx sc;
    c.srdA = mk_srd(A.ckv, 2048 * 512 * 2);
    c.srdB = mk_srd(grp ? A.wuv : A.wuk, 512 * 4096 * 2);
    c.srdW = mk_srd(A.wq, 512 * 4096 * 2);
#pragma unroll
    for (int par = 0; par < 2; ++par) {
      c.voffA[par] = (unsigned)((lane >> 3) * 1024 + (((lane & 7) ^ (4 * par + (lane >> 4))) * 16));
    }
    c.voffB = (unsigned)((lane >> 4) * 8192 + (((lane & 15) ^ (((lane >> 4) | ((w4 & 1) << 2)) << 1)) * 16));
    c.soffA = (unsigned)((pi * 256 + wid * 32) * 1024);
    c.soffB = (unsigned)((w4 * 8) * 8192 + head * 256);
    c.rot = 0;
    c.ldsA = ldsb + wid * 4096; c.ldsB = ldsb + 65536 + grp * 16384 + w4 * 2048;
    kv_dmaA<0, 0>(c, 0); kv_dmaB<0, 0>(c, 0); kv_dmaA<1, 0>(c, 0); kv_dmaB<1, 0>(c, 0); kv_dmaA<2, 0>(c, 0); kv_dmaA<3, 0>(c, 0); kv_dmaB<2, 0>(c, 0); kv_dmaB<3, 0>(c, 0);
    RAWBAR();
    sc.ep = gload1_sc1(sync);
    sc.xv = gload_asm(A.x + (size_t)(b * 8 + wid) * 512 + lane * 8);
    sc.xv2 = gload_asm(A.x + (size_t)(b * 8 + (lane & 7)) * 512 + (wid * 8 + (lane >> 3)) * 8);
    asm volatile("global_load_ushort %0, %1, off" : "=v"(sc.cv0) : "v"(A.c + (tid >> 1)) : "memory");
    asm volatile("global_load_ushort %0, %1, off" : "=v"(sc.cv1) : "v"(A.c + 256 + (tid >> 1)) : "memory");
    const u16* wmp = A.wmod + (size_t)(tid >> 1) * 3072 + (cb < 192 ? cb : 0) * 16 + (tid & 1) * 8;
    sc.wm0 = gload_asm(wmp); sc.wm1 = gload_asm(wmp + 256 * 3072);
    kv_dmaA<0, 1>(c, 1); kv_dmaA<1, 1>(c, 1); kv_dmaA<2, 1>(c, 1); kv_dmaA<3, 1>(c, 1); kv_dmaB<0, 1>(c, 1); kv_dmaB<1, 1>(c, 1); kv_dmaB<2, 1>(c, 1); kv_dmaB<3, 1>(c, 1);
    c.voffW = (unsigned)((lane >> 4) * 8192 + (((lane & 15) ^ (((lane >> 4) | ((wid & 1) << 2)) << 1)) * 16));
    c.soffW = (unsigned)(wid * 8 * 8192 + head * 256);
    c.ldsW = ldsb + wid * 2048;
#pragma unroll
    for (int ks = 0; ks < 2; ++ks) c.aaddr[ks] = ldsb + (w4 * 64 + c16) * 128 + (((4 * ks + g) ^ ((c16 >> 1) & 7)) * 16);
    { const int q = c16 >> 2, p = c16 & 3, f = q | ((g & 1) << 2);
#pragma unroll
      for (int dt = 0; dt < 8; ++dt) c.baddr[dt] = ldsb + 65536 + grp * 16384 + (8 * g + q) * 256 + ((dt ^ f) << 5) + 8 * p; }
    sc.gran = sync + 4096; sc.granw = sync + 4096 + cb * 64; sc.modw = (float*)(ws + WS_MOD) + cb * 16; sc.pub = wid == 7 && cb < 192;
    sc.tag = 0; sc.wid = wid; sc.lane = lane; sc.ldsx = ldsb + 131072;
    sc.hdst = ws + WS_H + (size_t)(b >> 1) * 16384 + (b & 1) * 128;
    sc.hflag_own = sync + 1024 + b * 4;
    sc.wqt = (unsigned)((pi * 64 + wid * 8) * 8192 + head * 256);
    if (wid == 1) {
      const unsigned long long lo = CODE_ADDR(k_front), hi = CODE_ADDR(k_front_end);
      dma4(mk_srd((const void*)lo, (unsigned)(hi - lo)), (lane & 31) * 64, idx * 2048, ldsb + 131072 + 3072);
      WAITV(9);
    } else
    if (wid == 3) {
      const int p = idx * 3 + (lane % 3);
      const char* q = p < 70 ? ws + (size_t)p * (1 << 20) : p < 74 ? (const char*)A.wq + (size_t)(p - 70) * (1 << 20) + 1048000 : p < 78 ? (const char*)A.pf0 + (size_t)(p - 74) * (1 << 20) + 1048000
                    : p < 81 ? (const char*)A.pf1 + (p - 78) * 917000 : p < 84 ? (const char*)A.pf2 + (p - 81) * 917000 : p < 87 ? (const char*)A.pf3 + (p - 84) * 917000
                    : p < 90 ? (const char*)A.out + (p - 87) * 1048000 : p < 92 ? (const char*)(p == 90 ? A.wq : A.pf0) : ws;
      asm volatile("s_mov_b32 m0, %0\n\ts_nop 3\n\tglobal_load_lds_dword %1, off" ::"s"(ldsb + 131072 + 3584), "v"(q) : "memory");
      WAITV(9);
    } else
    if (wid == 2) {
      dma4(mk_srd(ws, 1 << 20), lane * 64, b * 4096, ldsb + 131072 + 3328);
      WAITV(9);
    } else
    WAITV(8);
    side_partials(sc);
    BARRIER();
    if (sc.pub) {
      const int l2 = opaque(sc.lane);
      float t = 0.f;
#pragma unroll
      for (int wv = 0; wv < 8; ++wv) t += *(const LAS float*)(size_t)(sc.ldsx + wv * 64 + (l2 & 15) * 4);
      t = rbf(t);
      const float tn = xl1(t);
      if (l2 < 16) sc.modw[l2] = t;
      if (l2 < 16 && !(l2 & 1)) sstore2_sc1(sc.granw, l2 * 4, (u32x2){cvtpk(t, tn), sc.tag});
    }
    STAMP(1);
    const char* hneed = sync + 1024 + rb0 * 8;
    if (grp) kv_main<true>(c, sc, pk, hneed, h0, hp, dbg); else kv_main<false>(c, sc, pk, hneed, h0, hp, dbg);
    tag = sc.tag;
  }
  STAMP(2);
  const __amdgpu_buffer_rsrc_t srdKV = mk_srd(ws + WS_KV + (size_t)head * (1 << 20), 1 << 20);
  const unsigned voffL = lane * 16;
  const unsigned kvso = w4 * 4096;
  const unsigned ldsK = ldsb + kvso, ldsV = ldsK + 65536;
  AttState a;
  {
    u32x4 hf[4][2][2];
    hf[0][0][0] = h0[0]; hf[0][0][1] = h0[1]; hf[0][1][0] = h0[2]; hf[0][1][1] = h0[3];
#define QP_HL(j, i) hf[(j) & 3][(i) >> 1][(i) & 1] = gload_sc1(hp + ((i) & 1) * 16384 + (2 * (j) + ((i) >> 1)) * 1024)
#pragma unroll
    for (int j = 1; j < 3; ++j)
#pragma unroll
      for (int i = 0; i < 4; ++i) QP_HL(j, i);
    SBAR();
#define QP_ST(i) st16_wt(srdO, voffL + (i) * 1024, 0, pk[i])
    unsigned baddr[8];
    { const int q = c16 >> 2, p = c16 & 3, f = q | ((g & 1) << 2);
#pragma unroll
      for (int dt = 0; dt < 8; ++dt) baddr[dt] = ldsb + 131072 + (8 * g + q) * 256 + ((dt ^ f) << 5) + 8 * p; }
    bf16x8 wr[4];
#define QP_RDW(n) cat8(lds_tr(baddr[(n) & 7] + (((n) >> 4) & 1) * 16384 + (((n) >> 3) & 1) * 8192), lds_tr(baddr[(n) & 7] + (((n) >> 4) & 1) * 16384 + (((n) >> 3) & 1) * 8192 + 1024))
#define QP_WAIT(j, N, BAR) asm volatile("s_waitcnt vmcnt(" #N ")\n\t" BAR : "+v"(hf[(j) & 3][0][0]), "+v"(hf[(j) & 3][0][1]), "+v"(hf[(j) & 3][1][0]), "+v"(hf[(j) & 3][1][1])::"memory")
    QP_ST(0); QP_ST(1); QP_ST(2); SBAR();
    asm volatile("s_nop 1" : "+v"(hf[0][0][0]), "+v"(hf[0][0][1]), "+v"(hf[0][1][0]), "+v"(hf[0][1][1]));
    STAMP(3);
    wr[0] = QP_RDW(0); wr[1] = QP_RDW(1); wr[2] = QP_RDW(2); wr[3] = QP_RDW(3);
    SBAR();
#define QP_G(j, ks, dt, EXTRA) do { constexpr int n_ = 16 * (j) + 8 * (ks) + (dt); \
        if (n_ < 8) { mfma_zero(a.o[dt][0], wr[n_ & 3], __builtin_bit_cast(bf16x8, hf[0][0][0])); mfma_zero(a.o[dt][1], wr[n_ & 3], __builtin_bit_cast(bf16x8, hf[0][0][1])); } \
        else { mfma_acc(a.o[dt][0], wr[n_ & 3], __builtin_bit_cast(bf16x8, hf[(j) & 3][ks][0])); mfma_acc(a.o[dt][1], wr[n_ & 3], __builtin_bit_cast(bf16x8, hf[(j) & 3][ks][1])); } \
        if (n_ + 4 >= 32 && n_ + 4 < 128 && ((n_ + 4) & 31) < 8) { baddr[(n_ + 4) & 7] += qp_reg((n_ + 4) >> 4) - qp_reg(((n_ + 4) >> 4) - 2); PIN(baddr[(n_ + 4) & 7]); } \
        if (n_ + 4 < 128) wr[n_ & 3] = QP_RDW(n_ + 4); \
        EXTRA; SBAR(); } while (0)
#define QP_TILE(j, S0, NS, WAITNEXT) do { \
      QP_G(j, 0, 0, ); QP_G(j, 0, 1, if ((NS) > 0) QP_ST(((S0) + 0) & 15)); QP_G(j, 0, 2, ); QP_G(j, 0, 3, ); QP_G(j, 0, 4, if ((NS) > 1) QP_ST(((S0) + 1) & 15)); QP_G(j, 0, 5, ); QP_G(j, 0, 6, ); QP_G(j, 0, 7, if ((NS) > 2) QP_ST(((S0) + 2) & 15)); \
      QP_G(j, 1, 0, ); QP_G(j, 1, 1, if ((j) < 5) QP_HL((j) + 3, 0)); QP_G(j, 1, 2, ); QP_G(j, 1, 3, if ((j) < 5) QP_HL((j) + 3, 1)); QP_G(j, 1, 4, ); QP_G(j, 1, 5, if ((j) < 5) QP_HL((j) + 3, 2)); QP_G(j, 1, 6, ); QP_G(j, 1, 7, if ((j) < 5) QP_HL((j) + 3, 3)); \
      TLQ(); WAITNEXT; TLQ(); SBAR(); \
    } while (0)
#define TLQ()
    QP_TILE(0, 3, 3, QP_WAIT(1, 14, "s_barrier"));
    QP_TILE(1, 6, 3, QP_WAIT(2, 17, ""));
    QP_TILE(2, 9, 3, QP_WAIT(3, 14, ""));
    QP_TILE(3, 12, 2, QP_WAIT(4, 13, ""));
    QP_TILE(4, 14, 2, QP_WAIT(5, 12, ""));
    QP_TILE(5, 0, 0, QP_WAIT(6, 4, ""));
    if (grp == 0) { rt_load(A.pf0, A.pf1, b * 4 + w4, lane, ldsb + 98304 + w4 * 8192); rt_load(A.pf0, A.pf1, 1024 + b * 4 + w4, lane, ldsb + 98304 + w4 * 8192 + 4096); }
    RAWBAR();
    if (tid == 0) gstore1_sc1(sync + 2048 + (head * 8 + pi) * 4, tag);
    QP_TILE(6, 0, 0, if (grp == 1) WAITV(0); QP_WAIT(7, 8, ""));
    if (grp == 0) { const int bb = b < 224 ? b : 0, isu = bb >= 112, F = isu ? bb - 112 : bb;
      const __amdgpu_buffer_rsrc_t srdF = mk_srd(isu ? A.pf3 : A.pf2, 512 * 1792 * 2);
      const unsigned vo = (lane >> 1) * 3584 + (lane & 1) * 16, so = w4 * 32 * 3584 + F * 32;
#pragma unroll
      for (int i = 0; i < 4; ++i) dma16(srdF, vo, so + i * 128 * 3584, ldsb + 131072 + w4 * 4096 + i * 1024);
      dma16c(mk_srd(sync + 4096 + 6144, 4096), voffL, w4 * 1024, ldsb + 151552 + w4 * 1024); }
    QP_TILE(7, 0, 0, );
    MFMA_DRAIN();
#pragma unroll
    for (int qt = 0; qt < 2; ++qt)
#pragma unroll
      for (int ds = 0; ds < 4; ++ds) a.q[qt][ds] = __builtin_bit_cast(bf16x8, pack44(a.o[2 * ds][qt] * QSCALE, a.o[2 * ds + 1][qt] * QSCALE));
    STAMP(4);
  }
#pragma unroll
  for (int dt = 0; dt < 8; ++dt) { a.o[dt][0] = f32x4{}; a.o[dt][1] = f32x4{}; PIN(a.o[dt][0]); PIN(a.o[dt][1]); }
  a.negm[0] = f32x4{}; a.negm[1] = f32x4{}; PIN(a.negm[0]); PIN(a.negm[1]); a.l[0] = f32x4{}; a.l[1] = f32x4{}; PIN(a.l[0]); PIN(a.l[1]); a.m[0] = a.m[1] = 0.f;
  a.ones = __builtin_bit_cast(bf16x8, (u32x4){0x3F803F80u, 0x3F803F80u, 0x3F803F80u, 0x3F803F80u}); PIN(a.ones);
#pragma unroll
  for (int i = 0; i < 2; ++i) { a.p[i][0] = u32x4{}; a.p[i][1] = u32x4{}; PIN(a.p[i][0]); PIN(a.p[i][1]); }
  const unsigned rdK = ldsb + lane * 16, rdV = rdK + 65536;
#define ATT_TILE(s_) ((s_) < nA ? (s_) : 2 * (s_) - nA + grp)
  bool actPrev = wid == 3;
  char* const ctxw = ws + WS_CTX + ((size_t)(w4 * 2) * 128 + head * 4) * 1024;
  char* const cflag = sync + 16384 + (w4 * 32 + head) * 4;
  unsigned long long tri[4];
#pragma unroll
  for (int r = 0; r < 4; ++r) tri[r] = __builtin_amdgcn_ballot_w64(4 * g + r > c16);
#pragma unroll
  for (int i = 0; i < APF; ++i) a.f[i] = lds_rd128(rdK + i * 1024);
  int rel0, special, special2; unsigned kcur, vprev, knext, sok, sov, lk, lv;
  unsigned tb[6];
  { const int s_ = lane - 1, T_ = ATT_TILE(s_), T2_ = ATT_TILE(s_ + 2), T1_ = ATT_TILE(s_ + 1);
    const int r_ = ((grp == 0 && s_ < nA) ? blkA : blkB) * 128 + w4 * 32 - T_ * 64;
    const bool dk_ = s_ + 2 < 17 && (grp == 0 || s_ + 2 >= nA), dv1_ = s_ + 1 < 17 && s_ + 1 >= nA, dv2_ = grp == 1 && s_ + 2 < nA;
    const int Tv_ = dv2_ ? s_ + 2 : T1_;
    tb[0] = dk_ ? kvso + (unsigned)T2_ * 32768u : (1u << 20); tb[1] = (dv1_ || dv2_) ? kvso + (unsigned)Tv_ * 32768u + 16384u : (1u << 20);
    tb[2] = dk_ ? ldsK + (unsigned)(T2_ & 3) * 16384u : 0x30000u; tb[3] = (dv1_ || dv2_) ? ldsV + (unsigned)(Tv_ & 3) * 16384u : 0x30000u;
    tb[4] = (unsigned)r_;
    const unsigned sp1 = ((grp == 0 && s_ == nA) ? 9 : 0) | (r_ < 63 ? 2 : 0) | (s_ < 0 ? 4 : 0) | (s_ == 0 ? 8 : 0) | ((grp == 0 && s_ == 16 && nA < 16) ? 16 : 0);
    const unsigned sp2 = (((grp == 0 && s_ == nA - 1) || s_ < 0) ? 1 : 0) | ((grp == 1 && s_ == nA - 2) ? 2 : 0);
    tb[5] = sp1 | (sp2 << 8) | ((unsigned)(T_ & 3) << 14) | ((unsigned)(ATT_TILE(s_ - 1) & 3) << 16) | ((unsigned)(T1_ & 3) << 18);
#pragma unroll
    for (int i = 0; i < 6; ++i) PIN(tb[i]); }
#define ATT_SCALARS(s_) do { const int l_ = (s_) + 1; sok = __builtin_amdgcn_readlane(tb[0], l_); sov = __builtin_amdgcn_readlane(tb[1], l_); lk = __builtin_amdgcn_readlane(tb[2], l_); lv = __builtin_amdgcn_readlane(tb[3], l_); \
    rel0 = __builtin_amdgcn_readlane(tb[4], l_); const unsigned w_ = __builtin_amdgcn_readlane(tb[5], l_); \
    special = w_ & 31; special2 = (w_ >> 8) & 3; kcur = w_ & 0xc000u; vprev = (w_ >> 2) & 0xc000u; knext = (w_ >> 4) & 0xc000u; \
    asm volatile("" : "+s"(rel0), "+s"(kcur), "+s"(vprev), "+s"(knext), "+s"(sok), "+s"(sov), "+s"(lk), "+s"(lv), "+s"(special), "+s"(special2)); } while (0)
  ATT_SCALARS(-1);
  TL_DECL
#pragma clang loop unroll(disable)
  for (int s = -1; s < 17; ++s) {
    if (__builtin_expect(s == 0, 0)) {
      WAITV_BAR(8);
      STAMP(5);
#pragma unroll
      for (int i = 0; i < APF; ++i) a.f[i] = lds_rd128(rdK + i * 1024);
        if (wid == 6 && lane < 32 && b < 224) {
          float t = 0.f;
#pragma unroll
          for (int wv = 0; wv < 4; ++wv) t += *(const LAS float*)(size_t)(ldsb + 147456 + wv * 128 + lane * 4);
          const int isu = b >= 112, F = isu ? b - 112 : b;
          gstore2_sc1(ws + WS_CSB + (isu * 3584 + (lane >> 4) * 1792 + F * 16 + (lane & 15)) * 8, (u32x2){__builtin_bit_cast(unsigned, t), tag});
        }
      if (grp == 1) BARRIER();
    }
    TLX();
    const bool act = rel0 >= 0 && (s >= 0 || wid == 3);
    TLD();
    __builtin_amdgcn_s_setprio(XPRIO);
    __builtin_amdgcn_s_waitcnt(0xC07F);
    if (act) att_qk(a, rdK + kcur, rdV + vprev);
    if (!act) {
#pragma unroll
      for (int i = 0; i < APF; ++i) a.f[i] = lds_rd128(rdV + vprev + i * 1024);
    }
    SBAR();
    if (actPrev) att_pv<true>(a, rdV + vprev);
    __builtin_amdgcn_s_setprio(YPRIO);
    if (__builtin_expect(s < 0 && wid == 0, 0)) {
      const int need = (2 * blkB + 1) >> 2;
#pragma clang loop unroll(disable)
      for (unsigned spin = 0; spin < SPIN_CAP; ++spin) {
        unsigned kvf = gload1_sc1(sync + 2048 + (head * 8 + (lane & 7)) * 4);
        asm volatile("s_waitcnt vmcnt(0)" : "+v"(kvf)::"memory");
        if (all_lanes(kvf == tag || (lane & 7) > need)) break;
        __builtin_amdgcn_s_sleep(1);
      }
    }
    TLX(); TLD();
    WAITV_BAR(0); SBAR();
    TLX(); TLD();
    if (__builtin_expect(special != 0, 0)) {
      MFMA_DRAIN();
      if (special & 4) {
        { const unsigned so = kvso + grp * 16384, ld = ldsK + grp * 65536;
          dma16c<0>(srdKV, voffL, so, ld); dma16c<1024>(srdKV, voffL, so, ld); dma16c<2048>(srdKV, voffL, so, ld); dma16c<3072>(srdKV, voffL, so, ld); }
    if (grp == 0) {
      u32x2 sg[8];
      const int l2 = opaque(lane);
      const char* gp = sync + 4096 + 6144 + (w4 * 16 + (l2 >> 2)) * 8;
#pragma unroll
      for (int m = 0; m < 8; ++m) sg[m] = *(const LAS u32x2*)(size_t)(ldsb + 151552 + (w4 * 16 + (l2 >> 2)) * 8 + (m < 4 ? 2048 : 0) + (m & 3) * 512);
#pragma clang loop unroll(disable)
      for (unsigned spin = 0; spin < SPIN_CAP; ++spin) {
        if (all_lanes(sg[0][1] == tag && sg[1][1] == tag && sg[2][1] == tag && sg[3][1] == tag && sg[4][1] == tag && sg[5][1] == tag && sg[6][1] == tag && sg[7][1] == tag)) break;
        __builtin_amdgcn_s_sleep(2);
        asm volatile("global_load_dwordx2 %0, %8, off offset:2048 sc1\n\tglobal_load_dwordx2 %1, %8, off offset:2560 sc1\n\tglobal_load_dwordx2 %2, %8, off offset:3072 sc1\n\tglobal_load_dwordx2 %3, %8, off offset:3584 sc1\n\t"
                     "global_load_dwordx2 %4, %8, off sc1\n\tglobal_load_dwordx2 %5, %8, off offset:512 sc1\n\tglobal_load_dwordx2 %6, %8, off offset:1024 sc1\n\tglobal_load_dwordx2 %7, %8, off offset:1536 sc1\n\ts_waitcnt vmcnt(0)"
                     : "=&v"(sg[0]), "=&v"(sg[1]), "=&v"(sg[2]), "=&v"(sg[3]), "=&v"(sg[4]), "=&v"(sg[5]), "=&v"(sg[6]), "=&v"(sg[7]) : "v"(gp) : "memory");
      }
      const bool od = (l2 >> 1) & 1;
      float p[16];
#pragma unroll
      for (int i = 0; i < 16; ++i) p[i] = 0.f;
#pragma unroll
      for (int m = 0; m < 4; ++m) {
        const u32x4 sw = *(const LAS u32x4*)(size_t)(ldsb + 131072 + w4 * 4096 + m * 1024 + l2 * 16);
        const float av = rbf(1.f + (od ? bfhi(sg[m][0]) : bflo(sg[m][0]))), sb = od ? bfhi(sg[4 + m][0]) : bflo(sg[4 + m][0]);
#pragma unroll
        for (int i = 0; i < 4; ++i) { const float w0 = bflo(sw[i]), w1 = bfhi(sw[i]); p[2 * i] += av * w0; p[2 * i + 1] += av * w1; p[8 + 2 * i] += sb * w0; p[9 + 2 * i] += sb * w1; }
      }
#pragma unroll
      for (int i = 0; i < 8; ++i) { auto r = __builtin_amdgcn_permlane32_swap(__builtin_bit_cast(unsigned, p[i]), __builtin_bit_cast(unsigned, p[8 + i]), false, false); p[i] = __builtin_bit_cast(float, (unsigned)r[0]) + __builtin_bit_cast(float, (unsigned)r[1]); }
#pragma unroll
      for (int i = 0; i < 4; ++i) { auto r = __builtin_amdgcn_permlane16_swap(__builtin_bit_cast(unsigned, p[i]), __builtin_bit_cast(unsigned, p[4 + i]), false, false); p[i] = __builtin_bit_cast(float, (unsigned)r[0]) + __builtin_bit_cast(float, (unsigned)r[1]); }
#pragma unroll
      for (int i = 0; i < 4; ++i) { p[i] += xl2(p[i]); p[i] += xl1(xm7(p[i])); p[i] += xl1(xm15(p[i])); }
      if (c16 < 2) *(LAS f32x4*)(size_t)(ldsb + 147456 + w4 * 128 + (g >> 1) * 64 + (c16 * 8 + (g & 1) * 4) * 4) = (f32x4){p[0], p[1], p[2], p[3]};
    }
        if (grp == 1) { const int l2 = opaque(lane); rt_store(ws, b * 4 + w4, l2, ldsb + 98304 + w4 * 8192); rt_store(ws, 1024 + b * 4 + w4, l2, ldsb + 98304 + w4 * 8192 + 4096); }
      }
      TLD();
      if (s == 0 && tid == 256) gstore1_sc1(sync + 3072 + b * 4, tag);
      if ((special & 16) && lane == 0) gstore1_sc1(cflag + blkA * 512, tag);
      if (special & 1) {
        const float sc[2] = {__builtin_amdgcn_rcpf(a.l[0][0]), __builtin_amdgcn_rcpf(a.l[1][0])};
        att_store(a, sc, ctxw + (size_t)blkA * (8 * 128 * 1024), voffL);
      }
      if (special & 5) {
#pragma unroll
        for (int dt = 0; dt < 8; ++dt) { a.o[dt][0] = f32x4{}; a.o[dt][1] = f32x4{}; }
        a.l[0] = f32x4{}; a.l[1] = f32x4{};
      }
      if (special & 2) {
        const float NI = -__builtin_inff(); const f32x4 NI4 = {NI, NI, NI, NI};
#define TRI(kt, qt) _Pragma("unroll") for (int r = 0; r < 4; ++r) asm volatile("v_cndmask_b32 %0, %0, %1, %2" : "+v"(a.s[kt][qt][r]) : "v"(NI), "s"(tri[r]))
        if (rel0 == 0) { TRI(0, 0); TRI(1, 1); a.s[1][0] = NI4; a.s[2][0] = NI4; a.s[2][1] = NI4; a.s[3][1] = NI4; }
        else { TRI(2, 0); TRI(3, 1); }
        a.s[3][0] = NI4;
#undef TRI
      }
      if ((special & 12) == 8 && act) {
#pragma unroll
        for (int qt = 0; qt < 2; ++qt) {
          float mx = a.s[0][qt][0];
#pragma unroll
          for (int kt = 0; kt < 4; ++kt)
#pragma unroll
            for (int r = 0; r < 4; ++r) mx = __builtin_fmaxf(mx, a.s[kt][qt][r]);
          mx = red_g_max(mx, lane) + M_BIAS;
          const float dl = mx == -__builtin_inff() ? 0.f : mx;
          a.m[qt] = dl; a.negm[qt] = (f32x4){-dl, -dl, -dl, -dl};
#pragma unroll
          for (int kt = 0; kt < 4; ++kt) a.s[kt][qt] -= dl;
        }
      }
    }
    SBAR();
    TLD();
    float e[4][2][4];
#define GRPE(k, qt, D) do { D; _Pragma("unroll") for (int r = 0; r < 4; ++r) e[k][qt][r] = __builtin_amdgcn_exp2f(a.s[k][qt][r]); SBAR(); } while (0)
#define PACK(kp, qt) a.p[kp][qt] = (u32x4){cvtpk(e[2 * kp][qt][0], e[2 * kp][qt][1]), cvtpk(e[2 * kp][qt][2], e[2 * kp][qt][3]), cvtpk(e[2 * kp + 1][qt][0], e[2 * kp + 1][qt][1]), cvtpk(e[2 * kp + 1][qt][2], e[2 * kp + 1][qt][3])}
#define OR4(kp, qt) (a.p[kp][qt][0] | a.p[kp][qt][1] | a.p[kp][qt][2] | a.p[kp][qt][3])
    GRPE(0, 0, dma16cf<0>(srdKV, voffL, sok, lk));   GRPE(1, 0, dma16cn<1024>(srdKV, voffL, sok));
    GRPE(0, 1, dma16cn<2048>(srdKV, voffL, sok); PACK(0, 0)); GRPE(1, 1, dma16cn<3072>(srdKV, voffL, sok));
    GRPE(2, 0, dma16cf<0>(srdKV, voffL, sov, lv); PACK(0, 1));   GRPE(3, 0, dma16cn<1024>(srdKV, voffL, sov));
    GRPE(2, 1, dma16cn<2048>(srdKV, voffL, sov); PACK(1, 0)); GRPE(3, 1, dma16cn<3072>(srdKV, voffL, sov));
    PACK(1, 1);
    const unsigned orall = OR4(0, 0) | OR4(0, 1) | OR4(1, 0) | OR4(1, 1);
    const bool bad = act && s >= 0 && __builtin_amdgcn_ballot_w64((orall & 0x40004000u) != 0) != 0;
    if (__builtin_expect(bad, 0)) {
#pragma unroll
      for (int qt = 0; qt < 2; ++qt) {
        float mx = a.s[0][qt][0];
#pragma unroll
        for (int kt = 0; kt < 4; ++kt)
#pragma unroll
          for (int r = 0; r < 4; ++r) mx = __builtin_fmaxf(mx, a.s[kt][qt][r]);
        mx = red_g_max(mx, lane) + M_BIAS;
        const float dl = __builtin_fmaxf(mx, 0.f);
        a.m[qt] += dl;
        const float nm = -a.m[qt]; a.negm[qt] = (f32x4){nm, nm, nm, nm};
        const float al = __builtin_amdgcn_exp2f(-dl);
        a.l[qt] *= al;
#pragma unroll
        for (int dt = 0; dt < 8; ++dt) a.o[dt][qt] *= al;
#pragma unroll
        for (int kt = 0; kt < 4; ++kt)
#pragma unroll
          for (int r = 0; r < 4; ++r) e[kt][qt][r] = __builtin_amdgcn_exp2f(a.s[kt][qt][r] - dl);
      }
      PACK(0, 0); PACK(0, 1); PACK(1, 0); PACK(1, 1);
    }
#undef GRPE
#undef PACK
#undef OR4
    actPrev = act;
    TLD();
    if (__builtin_expect(special2, 0)) {
      if (special2 & 1) {
        if (s >= 0) {
#pragma unroll
          for (int i = 0; i < 8; ++i) a.q[i >> 2][i & 3] = lds_rd128(ldsb + 131072 + w4 * 8192 + i * 1024 + lane * 16);
        } else actPrev = false;
        a.negm[0] = f32x4{}; a.negm[1] = f32x4{}; a.m[0] = a.m[1] = 0.f;
      }
      if (special2 & 2) {
#pragma unroll
        for (int i = 0; i < 8; ++i) *(LAS bf16x8*)(size_t)(ldsb + 131072 + w4 * 8192 + i * 1024 + lane * 16) = a.q[i >> 2][i & 3];
        asm volatile("s_waitcnt lgkmcnt(0)" ::: "memory");
      }
    }
    SBAR();
#pragma unroll
    for (int i = 0; i < APF; ++i) a.f[i] = lds_rd128(rdK + knext + i * 1024);
    SBAR();
    ATT_SCALARS(s + 1);
    TLD();
    RAWBAR(); SBAR();
    TLD();
  }
  __builtin_amdgcn_s_setprio(0);
  u32x4 kvall = gload_sc1(sync + 3072 + lane * 16);
  if (actPrev) att_pv<false>(a, rdV + vprev);
  MFMA_DRAIN();
  STAMP(6);
  asm volatile("s_waitcnt vmcnt(0) lgkmcnt(0)\n\ts_barrier" : "+v"(kvall)::"memory");
  const bool kvok = all_lanes(kvall[0] == tag && kvall[1] == tag && kvall[2] == tag && kvall[3] == tag);
  const unsigned xo = ldsb + w4 * 32768 + lane * 16;
  if (grp == 0) {
    if (nA == 16 && lane == 0) gstore1_sc1(cflag + blkA * 512, tag);
#pragma unroll
    for (int dt = 0; dt < 8; ++dt)
#pragma unroll
      for (int qt = 0; qt < 2; ++qt) *(LAS f32x4*)(size_t)(xo + (dt * 2 + qt) * 1024) = a.o[dt][qt];
    *(LAS f32x4*)(size_t)(ldsb + 131072 + w4 * 8192 + lane * 16) = (f32x4){a.m[0], a.m[1], a.l[0][0], a.l[1][0]};
    BARRIER();
    if (b == 0 && wid == 0) {
      for (unsigned spin = 0; spin < SPIN_CAP; ++spin) {
        u32x4 f = gload_sc1(sync + 1024 + lane * 16);
        asm volatile("s_waitcnt vmcnt(0)" : "+v"(f)::"memory");
        if (all_lanes(f[0] == tag && f[1] == tag && f[2] == tag && f[3] == tag)) break;
        __builtin_amdgcn_s_sleep(2);
      }
      if (lane == 0) gstore1_sc1(sync, tag);
    }
  } else {
    const f32x4 ml = *(const LAS f32x4*)(size_t)(ldsb + 131072 + w4 * 8192 + lane * 16);
    float w1[2], w2[2];
#pragma unroll
    for (int qt = 0; qt < 2; ++qt) {
      const float l1 = a.l[qt][0], l2 = ml[2 + qt], m2 = l2 > 0.f ? ml[qt] : -__builtin_inff();
      const float M = __builtin_fmaxf(a.m[qt], m2);
      const float e1 = __builtin_amdgcn_exp2f(a.m[qt] - M), e2 = __builtin_amdgcn_exp2f(m2 - M);
      const float rl = __builtin_amdgcn_rcpf(e1 * l1 + e2 * l2);
      w1[qt] = e1 * rl; w2[qt] = e2 * rl;
    }
#pragma unroll
    for (int dt = 0; dt < 8; ++dt)
#pragma unroll
      for (int qt = 0; qt < 2; ++qt) { const f32x4 o2 = *(const LAS f32x4*)(size_t)(xo + (dt * 2 + qt) * 1024); a.o[dt][qt] = a.o[dt][qt] * w1[qt] + o2 * w2[qt]; }
    const float one[2] = {1.f, 1.f};
    att_store(a, one, ctxw + (size_t)blkB * (8 * 128 * 1024), voffL);
    WAITV(0);
    if (lane == 0) gstore1_sc1(cflag + blkB * 512, tag);
  }
  STAMP(7); STAMP_OUT(dbg, 2);
  tail_phases(A.x, A.pf2, A.pf3, (u16*)A.out, ws, ldsb, tag, kvok);
  asm volatile("k_front_end:");
}
extern "C" void kernel_launch(void* const* d_in, const int* in_sizes, int n_in, void* d_out, int out_size, void* d_ws, size_t ws_size, hipStream_t stream) {
  const u16* x = (const u16*)d_in[0]; const u16* ckv = (const u16*)d_in[1]; const u16* wq = (const u16*)d_in[2]; const u16* wuk = (const u16*)d_in[3]; const u16* wuv = (const u16*)d_in[4]; const u16* wo = (const u16*)d_in[5];
  const u16* wg = (const u16*)d_in[7]; const u16* wu = (const u16*)d_in[8]; const u16* wd = (const u16*)d_in[9]; const u16* c = (const u16*)d_in[10]; const u16* wmod = (const u16*)d_in[11];
  (void)hipFuncSetAttribute((const void*)k_front, hipFuncAttributeMaxDynamicSharedMemorySize, FR_LDS);
  FrontArgs fa = {x, ckv, wq, wuk, wuv, c, wmod, wo, wd, wg, wu, (const u16*)d_out, (char*)d_ws};
  k_front<<<256, 512, FR_LDS, stream>>>(fa);
}
```
